```python
import torch

def _softmax(x, axis=-1):
    return torch.softmax(x, dim=axis)
def _gelu(x):
    return torch.nn.functional.gelu(x, approximate="tanh")
def _silu(x):
    return torch.nn.functional.silu(x)
def _sigmoid(x):
    return torch.sigmoid(x)
def _layernorm(x, axis=-1, eps=1e-5):
    m = x.mean(dim=axis, keepdim=True)
    v = x.var(dim=axis, keepdim=True, unbiased=False)
    return (x - m) / torch.sqrt(v + eps)
def _scatter_add(tgt, idx, src, axis=0):
    return tgt.clone().index_add_(axis, idx, src)
def _segment_sum(src, seg, n):
    out = torch.zeros((n,) + src.shape[1:], dtype=src.dtype)
    return out.index_add_(0, seg, src)
def _pad(x, pw):
    flat = tuple(v for pair in reversed(pw) for v in pair)
    return torch.nn.functional.pad(x, flat)

kernel_name = 'sequential_preLN__compressed_kv_cla__dense_activation_relu__decode'

def setup_inputs():

    _w = (lambda a: (lambda t: t.to(torch.bfloat16) if t.is_floating_point() else t)(torch.as_tensor(a)))
    def _A_setup():
        B, H, Sq, Sk, D, M = 1, 32, 1, 8192, 128, 640
        Dc = 4 * D
        return {
            "x": _w((lambda *s: torch.randn(*s, dtype=torch.bfloat16) * 0.05)(B, Sq, M)),
            "c_kv": _w((lambda *s: torch.randn(*s, dtype=torch.bfloat16) * 0.4)(B, Sk, Dc)),
            "w_q": _w((lambda *s: torch.randn(*s, dtype=torch.bfloat16) * 3.2)(M, H, D) * (M ** -0.5)),
            "w_uk": _w((lambda *s: torch.randn(*s, dtype=torch.bfloat16) * 3.2)(Dc, H, D) * (Dc ** -0.5)),
            "w_uv": _w((lambda *s: torch.randn(*s, dtype=torch.bfloat16) * 0.4)(Dc, H, D) * (Dc ** -0.5)),
            "w_o": _w((lambda *s: torch.randn(*s, dtype=torch.bfloat16) * 0.4)(H, D, M) * ((H * D) ** -0.5)),
        }
    _A_in = _A_setup()

    def _B_setup():
        B, S, D, Dff = 1, 1, 640, 1728
        return {
            "x": _w((lambda *s: torch.randn(*s, dtype=torch.bfloat16) * 0.05)(B, S, D)),
            "w1": _w((lambda *s: torch.randn(*s, dtype=torch.bfloat16) * 0.05)(D, Dff)),
            "w2": _w((lambda *s: torch.randn(*s, dtype=torch.bfloat16) * 0.05)(Dff, D)),
        }
    _B_in = _B_setup()

    return {
        "A_x": _A_in["x"],
        "A_c_kv": _A_in["c_kv"],
        "A_w_q": _A_in["w_q"],
        "A_w_uk": _A_in["w_uk"],
        "A_w_uv": _A_in["w_uv"],
        "A_w_o": _A_in["w_o"],
        "B_x": _B_in["x"],
        "B_w1": _B_in["w1"],
        "B_w2": _B_in["w2"],
    }

def reference(A_x, A_c_kv, A_w_q, A_w_uk, A_w_uv, A_w_o, B_x, B_w1, B_w2):
    def _A_ref(x, c_kv, w_q, w_uk, w_uv, w_o):
        B, Sq, M = x.shape[0], x.shape[1], x.shape[2]
        Sk = c_kv.shape[1]
        H, D = w_q.shape[1], w_q.shape[2]
        q = torch.einsum("bqm,mhd->bhqd", x, w_q)
        k = torch.einsum("bkc,chd->bhkd", c_kv, w_uk)
        v = torch.einsum("bkc,chd->bhkd", c_kv, w_uv)
        scores = torch.einsum("bhqd,bhkd->bhqk", q, k) * (D ** -0.5)
        off = Sk - Sq
        qi = torch.arange(Sq)[:, None]
        kj = torch.arange(Sk)[None, :]
        cmask = kj <= (qi + off)
        scores = torch.where(cmask[None, None], scores, -1e9)
        p = _softmax(scores, axis=-1)
        ctx = torch.einsum("bhqk,bhkd->bhqd", p, v)
        return torch.einsum("bhqd,hdm->bqm", ctx, w_o)

    def _B_ref(x, w1, w2):
        h = torch.einsum("bsd,df->bsf", x, w1)
        zero = torch.zeros_like(h)
        h = torch.maximum(h, zero)
        return torch.einsum("bsf,fd->bsd", h, w2)

    x = A_x
    h = x + _A_ref(_layernorm(x, axis=-1), A_c_kv, A_w_q, A_w_uk, A_w_uv, A_w_o)
    h2 = h + _B_ref(_layernorm(h, axis=-1), B_w1, B_w2)
    return h2
```

```cpp
#include <hip/hip_runtime.h>
#include <hip/hip_bf16.h>
typedef unsigned short u16;
__device__ __forceinline__ float bf2f(u16 b){ return __builtin_bit_cast(float, (unsigned)b << 16); }
__device__ __forceinline__ u16 f2bf(float f){ unsigned u=__builtin_bit_cast(unsigned,f); u += 0x7FFFu + ((u>>16)&1u); return (u16)(u>>16);}
constexpr int H=32, SK=8192, D=128, M=640, DC=512, DFF=1728;
constexpr int NS=128;

__device__ float block_sum(float v, float* red){
  for(int o=32;o>0;o>>=1) v += __shfl_xor(v,o);
  int w=threadIdx.x>>6; int nw=blockDim.x>>6;
  __syncthreads();
  if((threadIdx.x&63)==0) red[w]=v;
  __syncthreads();
  float s=0; for(int i=0;i<nw;i++) s+=red[i];
  return s;
}
__global__ void k_q(const u16* x, const u16* wq, float* q){
  __shared__ float xn[M]; __shared__ float red[8];
  int t=threadIdx.x; float s=0;
  for(int i=t;i<M;i+=128) s+=bf2f(x[i]);
  float mean=block_sum(s,red)/M; float v=0;
  for(int i=t;i<M;i+=128){float d=bf2f(x[i])-mean; v+=d*d;}
  float var=block_sum(v,red)/M; float r=1.0f/sqrtf(var+1e-5f);
  for(int i=t;i<M;i+=128) xn[i]=(bf2f(x[i])-mean)*r;
  __syncthreads();
  int h=blockIdx.x; float a=0;
  for(int m=0;m<M;m++) a+=xn[m]*bf2f(wq[(size_t)m*H*D+h*D+t]);
  q[h*D+t]=a;
}
__global__ void k_qa(const u16* wuk, const float* q, float* qa){
  int h=blockIdx.x,c=threadIdx.x; float a=0;
  for(int d=0;d<D;d++) a+=q[h*D+d]*bf2f(wuk[(size_t)c*H*D+h*D+d]);
  qa[h*DC+c]=a*0.08838834764831845f;
}
__global__ void k_sc(const u16* ckv, const float* qa, float* sc){
  int k=blockIdx.x*64+threadIdx.x,h=blockIdx.y; float a=0;
  for(int c=0;c<DC;c++) a+=qa[h*DC+c]*bf2f(ckv[(size_t)k*DC+c]);
  sc[h*SK+k]=a;
}
__global__ void k_sm(float* sc){
  __shared__ float red[8];
  int h=blockIdx.x,t=threadIdx.x; float m=-1e30f;
  for(int k=t;k<SK;k+=256) m=fmaxf(m,sc[h*SK+k]);
  for(int o=32;o>0;o>>=1) m=fmaxf(m,__shfl_xor(m,o));
  if((t&63)==0) red[t>>6]=m; __syncthreads();
  m=fmaxf(fmaxf(red[0],red[1]),fmaxf(red[2],red[3]));
  float s=0; for(int k=t;k<SK;k+=256){float e=expf(sc[h*SK+k]-m); sc[h*SK+k]=e; s+=e;}
  s=block_sum(s,red); float inv=1.0f/s;
  for(int k=t;k<SK;k+=256) sc[h*SK+k]*=inv;
}
__global__ void k_pv(const u16* ckv, const float* p, float* part){
  int s=blockIdx.x,h=blockIdx.y,c=threadIdx.x; float a=0;
  for(int k=s*(SK/NS);k<(s+1)*(SK/NS);k++) a+=p[h*SK+k]*bf2f(ckv[(size_t)k*DC+c]);
  part[((size_t)h*NS+s)*DC+c]=a;
}
__global__ void k_ctx(const float* part, const u16* wuv, float* ctx){
  __shared__ float lat[DC];
  int h=blockIdx.x,c=threadIdx.x; float a=0;
  for(int s=0;s<NS;s++) a+=part[((size_t)h*NS+s)*DC+c];
  lat[c]=a; __syncthreads();
  if(c<D){ float b=0; for(int i=0;i<DC;i++) b+=lat[i]*bf2f(wuv[(size_t)i*H*D+h*D+c]); ctx[h*D+c]=b; }
}
__global__ void k_o(const float* ctx, const u16* wo, const u16* x, float* hh){
  int m=blockIdx.x*64+threadIdx.x; float a=0;
  for(int i=0;i<H*D;i++) a+=ctx[i]*bf2f(wo[(size_t)i*M+m]);
  hh[m]=a+bf2f(x[m]);
}
__global__ void k_f1(const float* hh, const u16* w1, float* f){
  __shared__ float xn[M]; __shared__ float red[8];
  int t=threadIdx.x; float s=0;
  for(int i=t;i<M;i+=64) s+=hh[i];
  float mean=block_sum(s,red)/M; float v=0;
  for(int i=t;i<M;i+=64){float d=hh[i]-mean; v+=d*d;}
  float var=block_sum(v,red)/M; float r=1.0f/sqrtf(var+1e-5f);
  for(int i=t;i<M;i+=64) xn[i]=(hh[i]-mean)*r;
  __syncthreads();
  int j=blockIdx.x*64+t; float a=0;
  for(int m=0;m<M;m++) a+=xn[m]*bf2f(w1[(size_t)m*DFF+j]);
  f[j]=fmaxf(a,0.f);
}
__global__ void k_f2(const float* hh, const float* f, const u16* w2, u16* out){
  int m=blockIdx.x*64+threadIdx.x; float a=0;
  for(int i=0;i<DFF;i++) a+=f[i]*bf2f(w2[(size_t)i*M+m]);
  out[m]=f2bf(a+hh[m]);
}
extern "C" void kernel_launch(void* const* d_in, const int* in_sizes, int n_in,
                              void* d_out, int out_size, void* d_ws, size_t ws_size,
                              hipStream_t stream) {
  const u16* x=(const u16*)d_in[0]; const u16* ckv=(const u16*)d_in[1]; const u16* wq=(const u16*)d_in[2];
  const u16* wuk=(const u16*)d_in[3]; const u16* wuv=(const u16*)d_in[4]; const u16* wo=(const u16*)d_in[5];
  const u16* w1=(const u16*)d_in[7]; const u16* w2=(const u16*)d_in[8];
  float* ws=(float*)d_ws;
  float* q=ws; float* qa=q+4096; float* sc=qa+16384; float* part=sc+H*SK; float* ctx=part+(size_t)H*NS*DC; float* hh=ctx+4096; float* f=hh+1024;
  k_q<<<32,128,0,stream>>>(x,wq,q);
  k_qa<<<32,512,0,stream>>>(wuk,q,qa);
  k_sc<<<dim3(128,32),64,0,stream>>>(ckv,qa,sc);
  k_sm<<<32,256,0,stream>>>(sc);
  k_pv<<<dim3(NS,32),512,0,stream>>>(ckv,sc,part);
  k_ctx<<<32,512,0,stream>>>(part,wuv,ctx);
  k_o<<<10,64,0,stream>>>(ctx,wo,x,hh);
  k_f1<<<27,64,0,stream>>>(hh,w1,f);
  k_f2<<<10,64,0,stream>>>(hh,f,w2,(u16*)d_out);
}
```

```cpp
#include <hip/hip_runtime.h>
#include <hip/hip_bf16.h>

typedef unsigned short u16;
typedef unsigned long long u64;
typedef unsigned v4u __attribute__((ext_vector_type(4)));
typedef unsigned v2u __attribute__((ext_vector_type(2)));
typedef float f32x4 __attribute__((ext_vector_type(4)));
typedef float f32x2 __attribute__((ext_vector_type(2)));
typedef short s16x8 __attribute__((ext_vector_type(8)));
typedef short s16x4 __attribute__((ext_vector_type(4)));
typedef __bf16 bf16x2_t __attribute__((ext_vector_type(2)));
#define LAS __attribute__((address_space(3)))
#define DI __device__ __forceinline__

#define STAMPS 0
#define KB1 5
#define WO_EARLY 1
#define FF_EARLY 3

constexpr int DM = 640, DFF = 1728;
constexpr unsigned SENT = 0xFFFFFFFFu;
constexpr unsigned SPIN_CAP = 1u << 15;
constexpr int AUX_SC1 = 16;

constexpr int WS_GEN = 0;
constexpr int WS_CLR = 32768;
constexpr int WS_QP = 65536;
constexpr int WS_QA = WS_QP + 262144;
constexpr int WS_ST = WS_QA + 32768;
constexpr int WS_S2 = WS_ST + 65536;
constexpr int WS_CP = WS_S2 + 4096;
constexpr int WS_HP = WS_CP + 131072;
constexpr int WS_FP = WS_HP + 16384;
constexpr int WS_PB = WS_FP + 331776;
constexpr int WS_OP = WS_PB + 524288;
constexpr int WS_END = WS_OP + 524288;
constexpr int WS_STAMP = 4 << 20;

constexpr int L_SM = 141312;
constexpr int L_RED = L_SM, L_QB = L_SM + 2560, L_SC = L_SM + 2816, L_FR = L_SM + 4864, L_FS = L_FR + 320, L_QO = L_FS + 64;
constexpr int L_VT = 0, L_PS = 65536, L_WO = 98304;
constexpr int UV_STRIDE = 1056;
constexpr int L_KT = 0, L_QA = 65536, L_UV = 98304, L_W1 = 0, L_W2 = 20480;
constexpr int L_W1E = 132096;
constexpr int LDS_BYTES = L_SM + 5632;

DI float u2f(unsigned u) { return __builtin_bit_cast(float, u); }
DI unsigned f2u(float f) { return __builtin_bit_cast(unsigned, f); }
DI float bflo(unsigned u) { return u2f(u << 16); }
DI float bfhi(unsigned u) { return u2f(u & 0xFFFF0000u); }
DI unsigned pack2(float a, float b) {
  f32x2 v = {a, b};
  bf16x2_t p = __builtin_convertvector(v, bf16x2_t);
  return __builtin_bit_cast(unsigned, p);
}
DI unsigned nosent(unsigned u) { return u < 0xFFFFFFFEu ? u : 0xFFFFFFFEu; }
template <int CTRL> DI float dppf(float v) {
  return u2f((unsigned)__builtin_amdgcn_update_dpp(0, (int)f2u(v), CTRL, 0xf, 0xf, true));
}
DI float rs16(float a, float b) { auto r = __builtin_amdgcn_permlane16_swap(f2u(a), f2u(b), false, false); unsigned x = r[0], y = r[1]; return u2f(x) + u2f(y); }
DI float rs32(float a, float b) { auto r = __builtin_amdgcn_permlane32_swap(f2u(a), f2u(b), false, false); unsigned x = r[0], y = r[1]; return u2f(x) + u2f(y); }
DI float sum_x16(float v) { return rs16(v, v); }
DI float sum_x32(float v) { return rs32(v, v); }
DI float max_x16(float v) { auto r = __builtin_amdgcn_permlane16_swap(f2u(v), f2u(v), false, false); unsigned a = r[0], b = r[1]; return fmaxf(u2f(a), u2f(b)); }
DI float max_x32(float v) { auto r = __builtin_amdgcn_permlane32_swap(f2u(v), f2u(v), false, false); unsigned a = r[0], b = r[1]; return fmaxf(u2f(a), u2f(b)); }
DI float row_sum(float v) { v += dppf<0xB1>(v); v += dppf<0x4E>(v); v += dppf<0x124>(v); v += dppf<0x128>(v); return v; }
DI float wave_sum(float v) { return sum_x32(sum_x16(row_sum(v))); }
DI float sum4(f32x4 d) { return (d[0] + d[1]) + (d[2] + d[3]); }
DI f32x4 mfma32(v4u a, v4u b, f32x4 c) {
  return __builtin_amdgcn_mfma_f32_16x16x32_bf16(__builtin_bit_cast(s16x8, a), __builtin_bit_cast(s16x8, b), c, 0, 0, 0);
}
DI f32x4 mfma16(v2u a, v2u b, f32x4 c) {
  return __builtin_amdgcn_mfma_f32_16x16x16bf16_1k(__builtin_bit_cast(s16x4, a), __builtin_bit_cast(s16x4, b), c, 0, 0, 0);
}
DI v2u trread(LAS char* p) { return __builtin_bit_cast(v2u, __builtin_amdgcn_ds_read_tr16_b64_v4i16((LAS s16x4*)p)); }
DI v4u trfrag(LAS char* p0, LAS char* p1) { v2u a = trread(p0), b = trread(p1); return v4u{a.x, a.y, b.x, b.y}; }
DI void bar() { asm volatile("s_waitcnt lgkmcnt(0)\n\ts_barrier" ::: "memory"); }
DI void drain() { asm volatile("s_waitcnt vmcnt(0)" ::: "memory"); }
template <int N> DI void waitvm() { asm volatile("s_waitcnt vmcnt(%0)" ::"n"(N) : "memory"); }
DI void cfence() { asm volatile("" ::: "memory"); }
#define SB() __builtin_amdgcn_sched_barrier(0)
DI void dma16(const void* base, unsigned voff, unsigned lds) {
  unsigned keep;
  asm volatile("s_mov_b32 %0, m0\n\ts_mov_b32 m0, %3\n\ts_nop 0\n\tglobal_load_lds_dwordx4 %1, %2\n\ts_mov_b32 m0, %0"
               : "=&s"(keep) : "v"(voff), "s"(base), "s"(lds) : "memory");
}
typedef __amdgpu_buffer_rsrc_t rsrc_t;
DI v4u ld4(rsrc_t r, int off) { return __builtin_amdgcn_raw_buffer_load_b128(r, off, 0, AUX_SC1); }
DI v2u ld2(rsrc_t r, int off) { return __builtin_amdgcn_raw_buffer_load_b64(r, off, 0, AUX_SC1); }
DI void st4(rsrc_t r, int off, v4u v) { __builtin_amdgcn_raw_buffer_store_b128(v, r, off, 0, AUX_SC1); }
DI void st2(rsrc_t r, int off, v2u v) { __builtin_amdgcn_raw_buffer_store_b64(v, r, off, 0, AUX_SC1); }
DI void st1(rsrc_t r, int off, unsigned v) { __builtin_amdgcn_raw_buffer_store_b32(v, r, off, 0, AUX_SC1); }
DI unsigned umax4(v4u v) { return max(max(v.x, v.y), max(v.z, v.w)); }
DI bool anyl(bool p) { return __builtin_amdgcn_ballot_w64(p) != 0ull; }
#define POLL_LOOP _Pragma("unroll 1") for (unsigned sp = 0; sp < SPIN_CAP; ++sp)

#define STAMP(i) do {} while (0)

DI void wait_clr(rsrc_t rs, int lane, unsigned elo, unsigned ehi) {
  POLL_LOOP {
    cfence();
    v2u a[4];
#pragma unroll
    for (int k = 0; k < 4; ++k) a[k] = ld2(rs, WS_CLR + (k * 64 + lane) * 128);
    bool bad = false;
#pragma unroll
    for (int k = 0; k < 4; ++k) bad |= (a[k].x != elo) | (a[k].y != ehi);
    if (!anyl(bad)) break;
    __builtin_amdgcn_s_sleep(4);
  }
  cfence();
}

__global__ void __launch_bounds__(512, 2)
mla_block_kernel(const u16* __restrict__ x, const u16* __restrict__ ckv, const u16* __restrict__ wq, const u16* __restrict__ wuk,
                 const u16* __restrict__ wuv, const u16* __restrict__ wo, const u16* __restrict__ w1, const u16* __restrict__ w2,
                 u16* __restrict__ out, char* ws) {
  __shared__ __attribute__((aligned(16))) char smem[LDS_BYTES];
  LAS char* L = (LAS char*)smem;
  const unsigned lbase = (unsigned)(size_t)L;
  asm volatile("" ::"s"(x), "s"(ckv), "s"(wq), "s"(wuk), "s"(wuv), "s"(wo), "s"(w1), "s"(w2), "s"(out), "s"(ws));
  const int tid = threadIdx.x, lane = tid & 63, wv = __builtin_amdgcn_readfirstlane(tid >> 6);
  const int l15 = lane & 15, g = lane >> 4, tq = l15 >> 2, tp = lane & 3;
  const int b = blockIdx.x, xcd = b & 7, slot = b >> 3, idx = slot & 15;
  const bool roleB = slot < 16;
  const rsrc_t rs = __builtin_amdgcn_make_buffer_rsrc((void*)ws, 0, WS_END, 0x00020000);
  const v4u S4 = {SENT, SENT, SENT, SENT};
  const v4u ones = {0x3F803F80u, 0x3F803F80u, 0x3F803F80u, 0x3F803F80u};
  const f32x4 Z4 = {0.f, 0.f, 0.f, 0.f};
  const int h = xcd * 4 + (idx >> 2), j = idx & 3;
  const int id = xcd * 16 + idx;
  const int sp_ = xcd * 2 + (idx >> 3), cj = idx & 7;
  const int fgrp = xcd + 8 * (idx >> 2), ft = fgrp * 4 + (idx & 3);
  const bool has_ffn = fgrp < 27;
  STAMP(0);

  unsigned elo = 0, ehi = 0;
  v4u uk[4] = {S4, S4, S4, S4};
  const char* ukp = (const char*)wuk + ((size_t)(128 * j + 16 * wv + l15) * 4096 + h * 128 + 8 * g) * 2;
  const int j8 = j + (roleB ? 0 : 4);
  if (wv < 5) {
    const int row0 = 80 * j8 + 16 * wv + g;
    unsigned xs[5];
#pragma unroll
    for (int i = 0; i < 5; ++i) xs[i] = ((const unsigned*)x)[lane + 64 * i];
    u16 xr[4];
#pragma unroll
    for (int i = 0; i < 4; ++i) xr[i] = x[row0 + 4 * i];
    SB();
    v4u wr[4];
    const char* wp = (const char*)wq + ((size_t)row0 * 4096 + h * 128 + 8 * l15) * 2;
#pragma unroll
    for (int i = 0; i < 4; ++i) wr[i] = *(const v4u*)(wp + (size_t)i * 4 * 8192);
    SB();
    v2u gen = ld2(rs, WS_GEN);
    if (roleB) {
#pragma unroll
      for (int t = 0; t < 4; ++t) uk[t] = *(const v4u*)(ukp + t * 64);
    }
    SB();
    float s = 0.f;
#pragma unroll
    for (int i = 0; i < 5; ++i) s += bflo(xs[i]) + bfhi(xs[i]);
    const float mean = wave_sum(s) * (1.0f / DM);
    float v = 0.f;
#pragma unroll
    for (int i = 0; i < 5; ++i) { float a = bflo(xs[i]) - mean, c = bfhi(xs[i]) - mean; v += a * a + c * c; }
    const float rstd = 1.0f / sqrtf(wave_sum(v) * (1.0f / DM) + 1e-5f);
    float xn[4];
#pragma unroll
    for (int i = 0; i < 4; ++i) xn[i] = (bflo(xr[i]) - mean) * rstd;
    SB();
    float acc[8];
#pragma unroll
    for (int k = 0; k < 8; ++k) acc[k] = 0.f;
#pragma unroll
    for (int i = 0; i < 4; ++i) {
      acc[0] += xn[i] * bflo(wr[i].x); acc[1] += xn[i] * bfhi(wr[i].x);
      acc[2] += xn[i] * bflo(wr[i].y); acc[3] += xn[i] * bfhi(wr[i].y);
      acc[4] += xn[i] * bflo(wr[i].z); acc[5] += xn[i] * bfhi(wr[i].z);
      acc[6] += xn[i] * bflo(wr[i].w); acc[7] += xn[i] * bfhi(wr[i].w);
      SB();
    }
    float r4[4];
#pragma unroll
    for (int k = 0; k < 4; ++k) r4[k] = rs32(acc[k], acc[k + 4]);
    *(LAS f32x2*)(L + L_RED + wv * 512 + (8 * l15 + 2 * g) * 4) = f32x2{rs16(r4[0], r4[2]), rs16(r4[1], r4[3])};
    elo = gen.x + 1u; ehi = gen.y + (elo == 0u ? 1u : 0u);
    STAMP(1);
  } else if (wv == 7) {
    if (roleB) {
      if (lane < 16) st4(rs, WS_QA + h * 1024 + j * 256 + lane * 16, S4);
      else if (lane < 24) st4(rs, WS_HP + id * 128 + (lane - 16) * 16, S4);
      else if (lane >= 32 && lane < 48 && cj == 0) st4(rs, WS_S2 + sp_ * 256 + (lane - 32) * 16, S4);
#pragma unroll
      for (int i = 0; i < 4; ++i) st4(rs, WS_OP + (sp_ * 32 + cj * 4 + i) * 1024 + lane * 16, S4);
    } else {
#pragma unroll
      for (int i = 0; i < 4; ++i) st4(rs, WS_PB + (id * 4 + i) * 1024 + lane * 16, S4);
      if (lane < 32) st4(rs, WS_ST + id * 512 + lane * 16, S4);
      st4(rs, WS_CP + idx * 8192 + xcd * 1024 + lane * 16, S4);
      if (has_ffn) {
#pragma unroll
        for (int i = 0; i < 3; ++i) st4(rs, WS_FP + ft * 3072 + i * 1024 + lane * 16, S4);
      }
    }
    v2u gen = ld2(rs, WS_GEN);
    drain();
    elo = gen.x + 1u; ehi = gen.y + (elo == 0u ? 1u : 0u);
    if (lane == 0) st2(rs, WS_CLR + b * 128, v2u{elo, ehi});
    STAMP(1);
    if (roleB) {
#pragma unroll
      for (int t = 0; t < 4; ++t) uk[t] = *(const v4u*)(ukp + t * 64);
    }
  } else if (roleB) {
    __builtin_amdgcn_s_sleep(3);
#pragma unroll
    for (int t = 0; t < 4; ++t) uk[t] = *(const v4u*)(ukp + t * 64);
  }
  bar();
  if (wv == 4) {
    float a0 = 0.f, a1 = 0.f;
#pragma unroll
    for (int w = 0; w < 5; ++w) { f32x2 t = *(LAS f32x2*)(L + L_RED + w * 512 + lane * 8); a0 += t.x; a1 += t.y; }
    st4(rs, WS_QP + ((h * 8 + j8) * 128 + 2 * lane) * 8, v4u{f2u(a0), elo, f2u(a1), elo});
    STAMP(2);
  }

  if (roleB) {
    if (wv == 4) {
      v4u p[8];
      POLL_LOOP {
        cfence();
#pragma unroll
        for (int k = 0; k < 8; ++k) p[k] = ld4(rs, WS_QP + ((h * 8 + k) * 128 + 2 * lane) * 8);
        bool bad = false;
#pragma unroll
        for (int k = 0; k < 8; ++k) bad |= (p[k].y != elo) | (p[k].w != elo);
        if (!anyl(bad)) break;
      }
      cfence();
      float q0 = 0.f, q1 = 0.f;
#pragma unroll
      for (int k = 0; k < 8; ++k) { q0 += u2f(p[k].x); q1 += u2f(p[k].z); }
      *(LAS unsigned*)(L + L_QB + lane * 4) = pack2(q0, q1);
      STAMP(3);
    }
    bar();
    {
      f32x4 d = Z4;
#pragma unroll
      for (int t = 0; t < 4; ++t) d = mfma32(uk[t], *(LAS v4u*)(L + L_QB + t * 64 + g * 16), d);
      const float sc = 0.08838834764831845f * 1.4426950408889634f;
      if (l15 == 0) *(LAS v2u*)(L + L_QO + wv * 32 + g * 8) = v2u{nosent(pack2(d[0] * sc, d[1] * sc)), nosent(pack2(d[2] * sc, d[3] * sc))};
    }
    bar();
    if (wv == 0) {
      if (lane < 16) st4(rs, WS_QA + h * 1024 + j * 256 + lane * 16, *(LAS v4u*)(L + L_QO + lane * 16));
    } else {
      __builtin_amdgcn_s_sleep(2);
    }
    STAMP(4);
    if (wv == 7) wait_clr(rs, lane, elo, ehi);
    {
      const int rl = lane >> 3;
      const unsigned vo0 = (unsigned)((512 * sp_ + wv * 64 + rl) * 1024 + cj * 128);
      const unsigned c0 = (unsigned)(((lane & 7) ^ (rl & 2)) * 16);
#pragma unroll
      for (int i = 0; i < 8; ++i) dma16(ckv, vo0 + i * 8192 + (c0 ^ ((i & 1) << 6)), lbase + L_VT + (wv * 8 + i) * 1024);
    }
    unsigned wooff[5];
#pragma unroll
    for (int i = 0; i < 5; ++i) {
      const int ch = i * 64 + lane, row = ch / 5, cc = ch - row * 5;
      wooff[i] = (unsigned)((512 * xcd + 64 * wv + row) * 1280 + idx * 80 + cc * 16);
    }
#pragma unroll
    for (int i = 0; i < WO_EARLY; ++i) dma16(wo, wooff[i], lbase + L_WO + (wv * 5 + i) * 1024);
    waitvm<WO_EARLY>();
    bar();
    STAMP(5);
    const int ct = wv & 3, ht = wv >> 2;
    v4u vf[16];
    {
      const int f = ((tq >> 1) & 1) | ((g & 1) << 1);
      LAS char* a = L + L_VT + (8 * g + tq) * 128 + ((ct ^ f) * 32) + tp * 8;
#pragma unroll
      for (int t = 0; t < 16; ++t) vf[t] = trfrag(a + t * 4096, a + t * 4096 + 512);
    }
    {
      v4u pf[4];
      v2u mm[4] = {v2u{0u, 0u}, v2u{0u, 0u}, v2u{0u, 0u}, v2u{0u, 0u}};
      const int pb = WS_PB + (sp_ * 32 + wv * 4) * 1024 + lane * 16;
      const int mo = WS_ST + (16 * sp_ + 4 * g) * 256 + (16 * wv + l15) * 8;
      POLL_LOOP {
        cfence();
        if (wv < 2) {
#pragma unroll
          for (int k = 0; k < 4; ++k) mm[k] = ld2(rs, mo + k * 256);
        }
#pragma unroll
        for (int k = 0; k < 4; ++k) pf[k] = ld4(rs, pb + k * 1024);
        unsigned mx = 0u;
#pragma unroll
        for (int k = 0; k < 4; ++k) mx = max(max(mx, umax4(pf[k])), max(mm[k].x, mm[k].y));
        if (!anyl(mx == SENT)) break;
      }
      cfence();
      STAMP(6);
#pragma unroll
      for (int k = 0; k < 4; ++k) *(LAS v4u*)(L + L_PS + (wv * 4 + k) * 1024 + lane * 16) = pf[k];
      if (wv < 2) {
        const float m0 = u2f(mm[0].x), m1 = u2f(mm[1].x), m2 = u2f(mm[2].x), m3 = u2f(mm[3].x);
        const float M = max_x32(max_x16(fmaxf(fmaxf(m0, m1), fmaxf(m2, m3))));
        f32x4 s4 = {__builtin_amdgcn_exp2f(m0 - M), __builtin_amdgcn_exp2f(m1 - M), __builtin_amdgcn_exp2f(m2 - M), __builtin_amdgcn_exp2f(m3 - M)};
        *(LAS f32x4*)(L + L_SC + g * 512 + (wv * 16 + l15) * 16) = s4;
        if (cj == 0) {
          const float Ls = sum_x32(sum_x16((s4[0] * u2f(mm[0].y) + s4[1] * u2f(mm[1].y)) + (s4[2] * u2f(mm[2].y) + s4[3] * u2f(mm[3].y))));
          if (lane < 16) st2(rs, WS_S2 + sp_ * 256 + (wv * 16 + lane) * 8, v2u{nosent(f2u(M)), nosent(f2u(Ls))});
        }
      }
    }
    bar();
    {
      v4u pfr[16];
      f32x4 scv[4];
#pragma unroll
      for (int t = 0; t < 4; ++t) pfr[t] = *(LAS v4u*)(L + L_PS + (t * 2 + ht) * 1024 + lane * 16);
#pragma unroll
      for (int k = 0; k < 4; ++k) scv[k] = *(LAS f32x4*)(L + L_SC + k * 512 + (ht * 16 + l15) * 16);
#pragma unroll
      for (int t = 4; t < 16; ++t) pfr[t] = *(LAS v4u*)(L + L_PS + (t * 2 + ht) * 1024 + lane * 16);
      SB();
      f32x4 acc0 = Z4, acc1 = Z4;
#pragma unroll
      for (int t = 0; t < 16; t += 2) {
        const f32x4 d0 = mfma32(vf[t], pfr[t], Z4);
        const f32x4 d1 = mfma32(vf[t + 1], pfr[t + 1], Z4);
        acc0 += d0 * scv[t >> 2][t & 3];
        acc1 += d1 * scv[t >> 2][(t & 3) + 1];
      }
      const f32x4 acc = acc0 + acc1;
      st2(rs, WS_OP + (sp_ * 32 + cj * 4 + ct) * 1024 + (ht * 16 + l15) * 32 + g * 8,
          v2u{nosent(pack2(acc[0], acc[1])), nosent(pack2(acc[2], acc[3]))});
      STAMP(7);
    }
#pragma unroll
    for (int i = WO_EARLY; i < 5; ++i) dma16(wo, wooff[i], lbase + L_WO + (wv * 5 + i) * 1024);
    float xr0 = 0.f, xr1 = 0.f;
    if (wv == 0 && xcd == 0) { const unsigned xv = ((const unsigned*)x)[idx * 20 + (lane < 20 ? lane : 0)]; xr0 = bflo(xv); xr1 = bfhi(xv); }
    drain();
    STAMP(8);
    v4u of[2][3];
#pragma unroll
    for (int k = 0; k < 2; ++k)
#pragma unroll
      for (int n = 0; n < 3; ++n) {
        LAS char* a = L + L_WO + (64 * wv + 32 * k + 8 * g + tq) * 80 + n * 32 + tp * 8;
        of[k][n] = trfrag(a, a + 320);
      }
    {
      v4u a0, a1;
      const int co = WS_CP + l15 * 8192 + (512 * xcd + 64 * wv + 8 * g) * 2;
      POLL_LOOP {
        cfence();
        a0 = ld4(rs, co); a1 = ld4(rs, co + 64);
        if (!anyl(max(umax4(a0), umax4(a1)) == SENT)) break;
      }
      cfence();
      STAMP(9);
      f32x4 d[3];
#pragma unroll
      for (int n = 0; n < 3; ++n) d[n] = mfma32(a0, of[0][n], Z4);
#pragma unroll
      for (int n = 0; n < 3; ++n) d[n] = mfma32(a1, of[1][n], d[n]);
      const float s0 = sum4(d[0]), s1 = sum4(d[1]), s2 = sum4(d[2]);
      *(LAS float*)(L + L_RED + wv * 256 + lane * 4) = rs16(rs32(s0, s2), rs32(s1, s1));
    }
    bar();
    if (wv == 0) {
      float v0 = xr0, v1 = xr1;
#pragma unroll
      for (int w = 0; w < 8; ++w) { const f32x2 t = *(LAS f32x2*)(L + L_RED + w * 256 + (lane & 31) * 8); v0 += t.x; v1 += t.y; }
      if (lane < 20) st1(rs, WS_HP + id * 128 + lane * 4, nosent(pack2(v0, v1)));
      STAMP(10);
    } else if (wv == 7 && idx < 5) {
      const int task = xcd * 5 + idx, rg = lane >> 2, cq = lane & 3;
      const int col = task * 16 + cq * 4, sl = col / 40;
      v2u hp;
      POLL_LOOP {
        cfence();
        hp = ld2(rs, WS_HP + ((rg & 7) * 16 + sl) * 128 + (col - sl * 40) * 2);
        if (!anyl(max(hp.x, hp.y) == SENT)) break;
      }
      if (rg >= 8) hp = v2u{0u, 0u};
      v4u fr[7];
      const int fo = WS_FP + rg * 3072 + xcd * 384 + idx * 64 + cq * 16;
      const int fo6 = rg < 12 ? fo + 6 * 16 * 3072 : fo;
      POLL_LOOP {
        cfence();
#pragma unroll
        for (int k = 0; k < 6; ++k) fr[k] = ld4(rs, fo + k * 16 * 3072);
        fr[6] = ld4(rs, fo6);
        unsigned mx = 0u;
#pragma unroll
        for (int k = 0; k < 7; ++k) mx = max(mx, umax4(fr[k]));
        if (!anyl(mx == SENT)) break;
      }
      cfence();
      STAMP(14);
      f32x4 a = {bflo(hp.x), bfhi(hp.x), bflo(hp.y), bfhi(hp.y)};
#pragma unroll
      for (int k = 0; k < 6; ++k) a += __builtin_bit_cast(f32x4, fr[k]);
      if (rg < 12) a += __builtin_bit_cast(f32x4, fr[6]);
      float v = rs16(rs32(a[0], a[2]), rs32(a[1], a[3]));
      v += dppf<0x124>(v); v += dppf<0x128>(v);
      if (l15 < 4) out[task * 16 + l15 * 4 + g] = (u16)(pack2(v, v) & 0xFFFFu);
      if (task == 0 && lane == 0) st2(rs, WS_GEN, v2u{elo, ehi});
      STAMP(15);
    }
  } else {
    if (wv < 4) {
      unsigned vo[2];
#pragma unroll
      for (int i = 0; i < 2; ++i) {
        const int lam = wv * 16 + i * 8 + (lane >> 3);
        const int key = (lam & 32) + 8 * ((lam & 15) >> 2) + 4 * ((lam >> 4) & 1) + (lam & 3);
        vo[i] = (unsigned)((64 * id + key) * 1024 + (((lane & 7) ^ ((lam >> 1) & 7)) * 16));
      }
#pragma unroll
      for (int cg = 0; cg < KB1; ++cg)
#pragma unroll
        for (int i = 0; i < 2; ++i) dma16(ckv, vo[i] + cg * 128, lbase + L_KT + cg * 8192 + (wv * 2 + i) * 1024);
      waitvm<2>();
      bar();
      __builtin_amdgcn_s_sleep(2);
#pragma unroll
      for (int cg = KB1; cg < 8; ++cg)
#pragma unroll
        for (int i = 0; i < 2; ++i) dma16(ckv, vo[i] + cg * 128, lbase + L_KT + cg * 8192 + (wv * 2 + i) * 1024);
      waitvm<2 * (8 - KB1)>();
      bar();
#pragma unroll
      for (int i = 0; i < 8; ++i) dma16(wuv, (unsigned)((32 * idx + wv * 8 + i) * 8192 + xcd * 1024 + lane * 16), lbase + L_UV + (wv * 8 + i) * UV_STRIDE);
      waitvm<8>();
      bar();
      STAMP(5);
      drain();
    } else {
      const int w4 = wv - 4;
      if (wv == 7) wait_clr(rs, lane, elo, ehi);
      bar();
      STAMP(3);
      {
        v4u qf[8];
        const int qo = WS_QA + ((w4 >> 1) * 16 + l15) * 1024 + (w4 & 1) * 512 + g * 16;
        POLL_LOOP {
          cfence();
#pragma unroll
          for (int k = 0; k < 8; ++k) qf[k] = ld4(rs, qo + k * 64);
          unsigned mx = 0u;
#pragma unroll
          for (int k = 0; k < 8; ++k) mx = max(mx, umax4(qf[k]));
          if (!anyl(mx == SENT)) break;
        }
        cfence();
#pragma unroll
        for (int k = 0; k < 8; ++k) *(LAS v4u*)(L + L_QA + (w4 * 8 + k) * 1024 + lane * 16) = qf[k];
      }
      STAMP(4);
      bar();
      const int kt = w4 >> 1, ht = w4 & 1;
      v4u qf[16];
#pragma unroll
      for (int k = 0; k < 16; ++k) qf[k] = *(LAS v4u*)(L + L_QA + (ht * 16 + k) * 1024 + lane * 16);
      LAS char* ka = L + L_KT + (32 * kt + l15) * 128 + ((g ^ (l15 >> 1)) * 16);
      LAS char* kb = L + L_KT + (32 * kt + l15) * 128 + (((4 + g) ^ (l15 >> 1)) * 16);
      f32x4 s0 = Z4, s1 = Z4;
      {
        v4u kf[KB1][4];
#pragma unroll
        for (int cg = 0; cg < KB1; ++cg) {
          kf[cg][0] = *(LAS v4u*)(ka + cg * 8192); kf[cg][1] = *(LAS v4u*)(ka + cg * 8192 + 2048);
          kf[cg][2] = *(LAS v4u*)(kb + cg * 8192); kf[cg][3] = *(LAS v4u*)(kb + cg * 8192 + 2048);
        }
        SB();
#pragma unroll
        for (int cg = 0; cg < KB1; ++cg) {
          s0 = mfma32(kf[cg][0], qf[2 * cg], s0); s1 = mfma32(kf[cg][1], qf[2 * cg], s1);
          s0 = mfma32(kf[cg][2], qf[2 * cg + 1], s0); s1 = mfma32(kf[cg][3], qf[2 * cg + 1], s1);
        }
        SB();
      }
      bar();
      STAMP(5);
      {
        v4u kf[8 - KB1][4];
#pragma unroll
        for (int cg = KB1; cg < 8; ++cg) {
          kf[cg - KB1][0] = *(LAS v4u*)(ka + cg * 8192); kf[cg - KB1][1] = *(LAS v4u*)(ka + cg * 8192 + 2048);
          kf[cg - KB1][2] = *(LAS v4u*)(kb + cg * 8192); kf[cg - KB1][3] = *(LAS v4u*)(kb + cg * 8192 + 2048);
        }
        SB();
#pragma unroll
        for (int cg = KB1; cg < 8; ++cg) {
          s0 = mfma32(kf[cg - KB1][0], qf[2 * cg], s0); s1 = mfma32(kf[cg - KB1][1], qf[2 * cg], s1);
          s0 = mfma32(kf[cg - KB1][2], qf[2 * cg + 1], s0); s1 = mfma32(kf[cg - KB1][3], qf[2 * cg + 1], s1);
        }
      }
      float m = fmaxf(fmaxf(fmaxf(s0[0], s0[1]), fmaxf(s0[2], s0[3])), fmaxf(fmaxf(s1[0], s1[1]), fmaxf(s1[2], s1[3])));
      m = max_x32(max_x16(m));
      v4u pk;
      pk.x = nosent(pack2(__builtin_amdgcn_exp2f(s0[0] - m), __builtin_amdgcn_exp2f(s0[1] - m)));
      pk.y = nosent(pack2(__builtin_amdgcn_exp2f(s0[2] - m), __builtin_amdgcn_exp2f(s0[3] - m)));
      pk.z = nosent(pack2(__builtin_amdgcn_exp2f(s1[0] - m), __builtin_amdgcn_exp2f(s1[1] - m)));
      pk.w = nosent(pack2(__builtin_amdgcn_exp2f(s1[2] - m), __builtin_amdgcn_exp2f(s1[3] - m)));
      const int tile = id * 2 + kt;
      st4(rs, WS_PB + (tile * 2 + ht) * 1024 + lane * 16, pk);
      float ls = ((bflo(pk.x) + bfhi(pk.x)) + (bflo(pk.y) + bfhi(pk.y))) + ((bflo(pk.z) + bfhi(pk.z)) + (bflo(pk.w) + bfhi(pk.w)));
      ls = sum_x32(sum_x16(ls));
      if (lane < 16) st2(rs, WS_ST + tile * 256 + (ht * 16 + lane) * 8, v2u{nosent(f2u(m)), nosent(f2u(ls))});
      STAMP(6);
    }
    bar();
    STAMP(7);
    unsigned fo1 = (unsigned)(((lane >> 1) + wv * 160) * (DFF * 2) + ft * 32 + (lane & 1) * 16);
    unsigned fo2 = (unsigned)(ft * 20480 + (wv - 4) * 5120 + lane * 16);
    constexpr int FE = FF_EARLY < 5 ? FF_EARLY : 5;
    if (has_ffn) {
#pragma unroll
      for (int i = 0; i < FE / 2; ++i) {
        if (wv < 4) dma16(w1, fo1 + i * 32 * DFF * 2, lbase + L_W1 + (wv * 5 + i) * 1024);
        else dma16(w2, fo2 + i * 1024, lbase + L_W1 + (wv * 5 + i) * 1024);
      }
    }
    v4u uf[4];
#pragma unroll
    for (int n = 0; n < 4; ++n) {
      LAS char* a = L + L_UV + (8 * g + tq) * UV_STRIDE + wv * 128 + n * 32 + tp * 8;
      uf[n] = trfrag(a, a + 4 * UV_STRIDE);
    }
    const int uh = xcd * 4 + (wv >> 1);
    float wsc[4];
    {
      v2u ml;
      POLL_LOOP {
        cfence();
        ml = ld2(rs, WS_S2 + l15 * 256 + uh * 8);
        if (!anyl(max(ml.x, ml.y) == SENT)) break;
      }
      cfence();
      const float ms = u2f(ml.x);
      float M = fmaxf(ms, dppf<0xB1>(ms)); M = fmaxf(M, dppf<0x4E>(M));
      M = fmaxf(M, dppf<0x124>(M)); M = fmaxf(M, dppf<0x128>(M));
      const float e = __builtin_amdgcn_exp2f(ms - M);
      const float wl = e / row_sum(e * u2f(ml.y));
#pragma unroll
      for (int k = 0; k < 4; ++k) wsc[k] = u2f((unsigned)__builtin_amdgcn_ds_bpermute((4 * g + k) * 4, (int)f2u(wl)));
    }
    STAMP(8);
    {
      v4u a0;
      const int oo = WS_OP + (l15 * 32 + idx * 2 + (g >> 1)) * 1024 + uh * 32 + (g & 1) * 16;
      POLL_LOOP {
        cfence();
        a0 = ld4(rs, oo);
        if (!anyl(umax4(a0) == SENT)) break;
      }
      cfence();
      STAMP(9);
      float c[4];
#pragma unroll
      for (int n = 0; n < 4; ++n) {
        const f32x4 d = mfma32(a0, uf[n], Z4);
        c[n] = (d[0] * wsc[0] + d[1] * wsc[1]) + (d[2] * wsc[2] + d[3] * wsc[3]);
      }
      const float cv = rs16(rs32(c[0], c[2]), rs32(c[1], c[3]));
      const float cn = dppf<0xB1>(cv);
      if ((lane & 1) == 0) st1(rs, WS_CP + idx * 8192 + xcd * 1024 + wv * 128 + lane * 2, nosent(pack2(cv, cn)));
      STAMP(10);
    }
    if (!has_ffn) return;
    bar();
#pragma unroll
    for (int i = FE / 2; i < 5; ++i) {
      if (wv < 4) dma16(w1, fo1 + i * 32 * DFF * 2, lbase + L_W1 + (wv * 5 + i) * 1024);
      else dma16(w2, fo2 + i * 1024, lbase + L_W1 + (wv * 5 + i) * 1024);
    }
    drain();
    bar();
    STAMP(11);
    v4u w1f[4];
    v2u w2f[5];
    f32x4 cs = Z4;
    {
      const int w5 = wv < 5 ? wv : 0;
      LAS char* a = L + L_W1 + (8 * g + tq) * 32 + tp * 8;
#pragma unroll
      for (int k = 0; k < 4; ++k) w1f[k] = trfrag(a + (w5 * 4 + k) * 1024, a + (w5 * 4 + k) * 1024 + 128);
#pragma unroll
      for (int k = 0; k < 5; ++k) w2f[k] = trread(L + L_W2 + (4 * g + tq) * 1280 + ((wv * 5 + k) * 16 + 4 * tp) * 2);
      f32x4 cs2 = Z4;
#pragma unroll
      for (int k = 0; k < 20; k += 2) {
        cs = mfma32(trfrag(a + k * 1024, a + k * 1024 + 128), ones, cs);
        cs2 = mfma32(trfrag(a + k * 1024 + 1024, a + k * 1024 + 1152), ones, cs2);
      }
      cs += cs2;
    }
    if (wv < 5) {
      v4u h0, h1;
      const int ch0 = 16 * wv + 4 * (l15 >> 3) + g, ch1 = ch0 + 8;
      const int ho0 = WS_HP + ((l15 & 7) * 16 + ch0 / 5) * 128 + (ch0 % 5) * 16;
      const int ho1 = WS_HP + ((l15 & 7) * 16 + ch1 / 5) * 128 + (ch1 % 5) * 16;
      POLL_LOOP {
        cfence();
        h0 = ld4(rs, ho0); h1 = ld4(rs, ho1);
        if (!anyl(max(umax4(h0), umax4(h1)) == SENT)) break;
      }
      cfence();
      STAMP(12);
      f32x4 gm = mfma32(h0, h0, Z4);
      f32x4 da = mfma32(h0, w1f[0], Z4), db = mfma32(h0, w1f[1], Z4);
      gm = mfma32(h1, h1, gm);
      f32x4 sm = mfma32(h0, ones, Z4);
      da = mfma32(h1, w1f[2], da); db = mfma32(h1, w1f[3], db);
      sm = mfma32(h1, ones, sm);
      const float gs = row_sum(((l15 < 8) == (g < 2)) ? sum4(gm) : 0.f);
      const float hv = g < 2 ? sum4(da) : sum4(db);
      const float r = rs16(rs32(hv, gs), rs32(hv, sum4(sm)));
      if (lane < 16) *(LAS float*)(L + L_FR + wv * 64 + lane * 4) = r;
      if (lane == 32 || lane == 48) *(LAS float*)(L + L_FS + (g - 2) * 32 + wv * 4) = r;
    }
    bar();
    {
      f32x4 hw = Z4;
#pragma unroll
      for (int w = 0; w < 5; ++w) hw += *(LAS f32x4*)(L + L_FR + w * 64 + g * 16);
      const f32x4 qa = *(LAS f32x4*)(L + L_FS), sa = *(LAS f32x4*)(L + L_FS + 32);
      const float q4 = *(LAS float*)(L + L_FS + 16), s4 = *(LAS float*)(L + L_FS + 48);
      const float mean = (sum4(sa) + s4) * (1.0f / DM);
      const float var = fmaxf((sum4(qa) + q4) * (1.0f / DM) - mean * mean, 0.f);
      const float rstd = __builtin_amdgcn_rsqf(var + 1e-5f);
      float f[4];
#pragma unroll
      for (int k = 0; k < 4; ++k) f[k] = fmaxf((hw[k] - mean * cs[k]) * rstd, 0.f);
      const v2u fb = {pack2(f[0], f[1]), pack2(f[2], f[3])};
      f32x4 o = Z4;
#pragma unroll
      for (int k = 0; k < 5; ++k) {
        const f32x4 d = mfma16(w2f[k], fb, Z4);
        if (l15 == k) o = d;
      }
      if (l15 < 5)
        st4(rs, WS_FP + ft * 3072 + wv * 384 + l15 * 64 + g * 16, v4u{nosent(f2u(o[0])), nosent(f2u(o[1])), nosent(f2u(o[2])), nosent(f2u(o[3]))});
      STAMP(13);
    }
  }
}

extern "C" void kernel_launch(void* const* d_in, const int* in_sizes, int n_in, void* d_out, int out_size, void* d_ws, size_t ws_size,
                              hipStream_t stream) {
  mla_block_kernel<<<256, 512, 0, stream>>>((const u16*)d_in[0], (const u16*)d_in[1], (const u16*)d_in[2], (const u16*)d_in[3],
                                            (const u16*)d_in[4], (const u16*)d_in[5], (const u16*)d_in[7], (const u16*)d_in[8],
                                            (u16*)d_out, (char*)d_ws);
}
```

```cpp
#include <hip/hip_runtime.h>
#include <hip/hip_bf16.h>

typedef unsigned short u16;
typedef unsigned long long u64;
typedef unsigned v4u __attribute__((ext_vector_type(4)));
typedef unsigned v2u __attribute__((ext_vector_type(2)));
typedef float f32x4 __attribute__((ext_vector_type(4)));
typedef float f32x2 __attribute__((ext_vector_type(2)));
typedef short s16x8 __attribute__((ext_vector_type(8)));
typedef short s16x4 __attribute__((ext_vector_type(4)));
typedef __bf16 bf16x2_t __attribute__((ext_vector_type(2)));
#define LAS __attribute__((address_space(3)))
#define DI __device__ __forceinline__

#define STAMPS 0
#define KB1 5
#define WO_EARLY 1
#define W2_EARLY 1

constexpr int DM = 640, DFF = 1728;
constexpr unsigned SENT = 0xFFFFFFFFu;
constexpr unsigned SPIN_CAP = 1u << 15;
constexpr int AUX_SC1 = 16;

constexpr int WS_GEN = 0;
constexpr int WS_CLR = 32768;
constexpr int WS_QP = 65536;
constexpr int WS_QA = WS_QP + 262144;
constexpr int WS_ST = WS_QA + 32768;
constexpr int WS_S2 = WS_ST + 65536;
constexpr int WS_CP = WS_S2 + 4096;
constexpr int WS_HP = WS_CP + 131072;
constexpr int WS_FP = WS_HP + 16384;
constexpr int WS_PB = WS_FP + 331776;
constexpr int WS_OP = WS_PB + 524288;
constexpr int WS_END = WS_OP + 524288;
constexpr int WS_STAMP = 4 << 20;

constexpr int L_SM = 153088;
constexpr int L_RED = L_SM, L_QB = L_SM + 2560, L_MW = L_SM + 2816, L_FR = L_SM + 3840, L_FS = L_FR + 320, L_QO = L_FS + 64, L_CS = L_QO + 256;
constexpr int L_VT = 0, L_RB = 65536, L_UK = 65536;
constexpr int UV_STRIDE = 1056, QA_STRIDE = 1040;
constexpr int L_KT = 0, L_QA = 65536, L_UV = 98816, L_W2 = 132608, L_W1 = 0;
constexpr int LDS_BYTES = L_SM + 5120;

DI float u2f(unsigned u) { return __builtin_bit_cast(float, u); }
DI unsigned f2u(float f) { return __builtin_bit_cast(unsigned, f); }
DI float bflo(unsigned u) { return u2f(u << 16); }
DI float bfhi(unsigned u) { return u2f(u & 0xFFFF0000u); }
DI unsigned pack2(float a, float b) {
  f32x2 v = {a, b};
  bf16x2_t p = __builtin_convertvector(v, bf16x2_t);
  return __builtin_bit_cast(unsigned, p);
}
DI unsigned nosent(unsigned u) { return u < 0xFFFFFFFEu ? u : 0xFFFFFFFEu; }
template <int CTRL> DI float dppf(float v) {
  return u2f((unsigned)__builtin_amdgcn_update_dpp(0, (int)f2u(v), CTRL, 0xf, 0xf, true));
}
DI float rs16(float a, float b) { auto r = __builtin_amdgcn_permlane16_swap(f2u(a), f2u(b), false, false); unsigned x = r[0], y = r[1]; return u2f(x) + u2f(y); }
DI float rs32(float a, float b) { auto r = __builtin_amdgcn_permlane32_swap(f2u(a), f2u(b), false, false); unsigned x = r[0], y = r[1]; return u2f(x) + u2f(y); }
DI float sum_x16(float v) { return rs16(v, v); }
DI float sum_x32(float v) { return rs32(v, v); }
DI float max_x16(float v) { auto r = __builtin_amdgcn_permlane16_swap(f2u(v), f2u(v), false, false); unsigned a = r[0], b = r[1]; return fmaxf(u2f(a), u2f(b)); }
DI float max_x32(float v) { auto r = __builtin_amdgcn_permlane32_swap(f2u(v), f2u(v), false, false); unsigned a = r[0], b = r[1]; return fmaxf(u2f(a), u2f(b)); }
DI float row_sum(float v) { v += dppf<0xB1>(v); v += dppf<0x4E>(v); v += dppf<0x124>(v); v += dppf<0x128>(v); return v; }
DI float wave_sum(float v) { return sum_x32(sum_x16(row_sum(v))); }
DI float sum4(f32x4 d) { return (d[0] + d[1]) + (d[2] + d[3]); }
DI f32x4 mfma32(v4u a, v4u b, f32x4 c) {
  return __builtin_amdgcn_mfma_f32_16x16x32_bf16(__builtin_bit_cast(s16x8, a), __builtin_bit_cast(s16x8, b), c, 0, 0, 0);
}
DI f32x4 mfma16(v2u a, v2u b, f32x4 c) {
  return __builtin_amdgcn_mfma_f32_16x16x16bf16_1k(__builtin_bit_cast(s16x4, a), __builtin_bit_cast(s16x4, b), c, 0, 0, 0);
}
DI v2u trread(LAS char* p) { return __builtin_bit_cast(v2u, __builtin_amdgcn_ds_read_tr16_b64_v4i16((LAS s16x4*)p)); }
DI v4u trfrag(LAS char* p0, LAS char* p1) { v2u a = trread(p0), b = trread(p1); return v4u{a.x, a.y, b.x, b.y}; }
DI void bar() { asm volatile("s_waitcnt lgkmcnt(0)\n\ts_barrier" ::: "memory"); }
DI void drain() { asm volatile("s_waitcnt vmcnt(0)" ::: "memory"); }
template <int N> DI void waitvm() { asm volatile("s_waitcnt vmcnt(%0)" ::"n"(N) : "memory"); }
DI void cfence() { asm volatile("" ::: "memory"); }
#define SB() __builtin_amdgcn_sched_barrier(0)
DI void dma16(const void* base, unsigned voff, unsigned lds) {
  unsigned keep;
  asm volatile("s_mov_b32 %0, m0\n\ts_mov_b32 m0, %3\n\ts_nop 0\n\tglobal_load_lds_dwordx4 %1, %2\n\ts_mov_b32 m0, %0"
               : "=&s"(keep) : "v"(voff), "s"(base), "s"(lds) : "memory");
}
typedef __amdgpu_buffer_rsrc_t rsrc_t;
DI v4u ld4(rsrc_t r, int off) { return __builtin_amdgcn_raw_buffer_load_b128(r, off, 0, AUX_SC1); }
DI v2u ld2(rsrc_t r, int off) { return __builtin_amdgcn_raw_buffer_load_b64(r, off, 0, AUX_SC1); }
DI void st4(rsrc_t r, int off, v4u v) { __builtin_amdgcn_raw_buffer_store_b128(v, r, off, 0, AUX_SC1); }
DI void st2(rsrc_t r, int off, v2u v) { __builtin_amdgcn_raw_buffer_store_b64(v, r, off, 0, AUX_SC1); }
DI void st1(rsrc_t r, int off, unsigned v) { __builtin_amdgcn_raw_buffer_store_b32(v, r, off, 0, AUX_SC1); }
DI unsigned umax4(v4u v) { return max(max(v.x, v.y), max(v.z, v.w)); }
DI bool anyl(bool p) { return __builtin_amdgcn_ballot_w64(p) != 0ull; }
#define POLL_LOOP _Pragma("unroll 1") for (unsigned sp = 0; sp < SPIN_CAP; ++sp)

#define STAMP(i) do {} while (0)

DI void wait_clr(rsrc_t rs, int lane, unsigned elo, unsigned ehi) {
  POLL_LOOP {
    cfence();
    v2u a[4];
#pragma unroll
    for (int k = 0; k < 4; ++k) a[k] = ld2(rs, WS_CLR + (k * 64 + lane) * 128);
    bool bad = false;
#pragma unroll
    for (int k = 0; k < 4; ++k) bad |= (a[k].x != elo) | (a[k].y != ehi);
    if (!anyl(bad)) break;
    __builtin_amdgcn_s_sleep(4);
  }
  cfence();
}

__global__ void __launch_bounds__(512, 2)
mla_block_kernel(const u16* __restrict__ x, const u16* __restrict__ ckv, const u16* __restrict__ wq, const u16* __restrict__ wuk,
                 const u16* __restrict__ wuv, const u16* __restrict__ wo, const u16* __restrict__ w1, const u16* __restrict__ w2,
                 u16* __restrict__ out, char* ws) {
  __shared__ __attribute__((aligned(16))) char smem[LDS_BYTES];
  LAS char* L = (LAS char*)smem;
  const unsigned lbase = (unsigned)(size_t)L;
  asm volatile("" ::"s"(x), "s"(ckv), "s"(wq), "s"(wuk), "s"(wuv), "s"(wo), "s"(w1), "s"(w2), "s"(out), "s"(ws));
  const int tid = threadIdx.x, lane = tid & 63, wv = __builtin_amdgcn_readfirstlane(tid >> 6);
  const int l15 = lane & 15, g = lane >> 4, tq = l15 >> 2, tp = lane & 3;
  const int b = blockIdx.x, xcd = b & 7, slot = b >> 3, idx = slot & 15;
  const bool roleB = slot < 16;
  const rsrc_t rs = __builtin_amdgcn_make_buffer_rsrc((void*)ws, 0, WS_END, 0x00020000);
  const v4u S4 = {SENT, SENT, SENT, SENT};
  const v4u ones = {0x3F803F80u, 0x3F803F80u, 0x3F803F80u, 0x3F803F80u};
  const f32x4 Z4 = {0.f, 0.f, 0.f, 0.f};
  const int h = xcd * 4 + (idx >> 2), j = idx & 3;
  const int id = xcd * 16 + idx;
  const int sp_ = xcd * 2 + (idx >> 3), cj = idx & 7;
  const int fgrp = xcd + 8 * (idx >> 2), ft = fgrp * 4 + (idx & 3);
  const bool has_ffn = fgrp < 27;
  STAMP(0);

  unsigned elo = 0, ehi = 0;
  v2u gen = {0u, 0u};
  const int j8 = j + (roleB ? 0 : 4);
  if (wv < 5) {
    const int row0 = 80 * j8 + 16 * wv + 4 * g;
    unsigned xs[5];
#pragma unroll
    for (int i = 0; i < 5; ++i) xs[i] = ((const unsigned*)x)[lane + 64 * i];
    const v2u xr = *(const v2u*)(x + row0);
    SB();
    v4u wr[4];
    const char* wp = (const char*)wq + (unsigned)(row0 * 8192 + h * 256 + 16 * l15);
#pragma unroll
    for (int i = 0; i < 4; ++i) wr[i] = *(const v4u*)(wp + i * 8192);
    SB();
    if (wv == 4) gen = ld2(rs, WS_GEN);
    SB();
    float s = 0.f;
#pragma unroll
    for (int i = 0; i < 5; ++i) s += bflo(xs[i]) + bfhi(xs[i]);
    const float mean = wave_sum(s) * (1.0f / DM);
    float v = 0.f;
#pragma unroll
    for (int i = 0; i < 5; ++i) { float a = bflo(xs[i]) - mean, c = bfhi(xs[i]) - mean; v += a * a + c * c; }
    const float rstd = __builtin_amdgcn_rsqf(wave_sum(v) * (1.0f / DM) + 1e-5f);
    const float xn[4] = {(bflo(xr.x) - mean) * rstd, (bfhi(xr.x) - mean) * rstd, (bflo(xr.y) - mean) * rstd, (bfhi(xr.y) - mean) * rstd};
    SB();
    float acc[8];
#pragma unroll
    for (int k = 0; k < 8; ++k) acc[k] = 0.f;
#pragma unroll
    for (int i = 0; i < 4; ++i) {
      acc[0] += xn[i] * bflo(wr[i].x); acc[1] += xn[i] * bfhi(wr[i].x);
      acc[2] += xn[i] * bflo(wr[i].y); acc[3] += xn[i] * bfhi(wr[i].y);
      acc[4] += xn[i] * bflo(wr[i].z); acc[5] += xn[i] * bfhi(wr[i].z);
      acc[6] += xn[i] * bflo(wr[i].w); acc[7] += xn[i] * bfhi(wr[i].w);
      SB();
    }
    float r4[4];
#pragma unroll
    for (int k = 0; k < 4; ++k) r4[k] = rs32(acc[k], acc[k + 4]);
    *(LAS f32x2*)(L + L_RED + wv * 512 + (8 * l15 + 2 * g) * 4) = f32x2{rs16(r4[0], r4[2]), rs16(r4[1], r4[3])};
    STAMP(1);
  } else if (wv == 7) {
    if (roleB) {
      if (lane < 16) st4(rs, WS_QA + h * 1024 + j * 256 + lane * 16, S4);
      else if (lane < 24 && idx < 10) st4(rs, WS_HP + xcd * 1280 + idx * 128 + (lane - 16) * 16, S4);
#pragma unroll
      for (int i = 0; i < 4; ++i) st4(rs, WS_OP + (sp_ * 32 + cj * 4 + i) * 1024 + lane * 16, S4);
    } else {
#pragma unroll
      for (int i = 0; i < 4; ++i) st4(rs, WS_PB + (id * 4 + i) * 1024 + lane * 16, S4);
      if (lane < 32) st4(rs, WS_ST + id * 512 + lane * 16, S4);
      else if (lane < 48 && cj == 0) st4(rs, WS_S2 + sp_ * 256 + (lane - 32) * 16, S4);
      st4(rs, WS_CP + idx * 8192 + xcd * 1024 + lane * 16, S4);
      if (has_ffn) {
#pragma unroll
        for (int i = 0; i < 3; ++i) st4(rs, WS_FP + ft * 3072 + i * 1024 + lane * 16, S4);
      }
    }
    gen = ld2(rs, WS_GEN);
  } else if (roleB) {
    __builtin_amdgcn_s_sleep(2);
    const unsigned uo = (unsigned)((128 * j + (wv - 5) * 64 + g) * 8192 + h * 256);
#pragma unroll
    for (int i = 0; i < 16; ++i) dma16(wuk, uo + i * 32768 + ((l15 ^ ((4 * i + g) & 15)) * 16), lbase + L_UK + ((wv - 5) * 16 + i) * 1024);
  }
  bar();
  if (wv == 4) {
    float a0 = 0.f, a1 = 0.f;
#pragma unroll
    for (int w = 0; w < 5; ++w) { f32x2 t = *(LAS f32x2*)(L + L_RED + w * 512 + lane * 8); a0 += t.x; a1 += t.y; }
    elo = gen.x + 1u;
    st4(rs, WS_QP + ((h * 8 + j8) * 128 + 2 * lane) * 8, v4u{f2u(a0), elo, f2u(a1), elo});
    STAMP(2);
  } else if (wv == 7) {
    drain();
    elo = gen.x + 1u; ehi = gen.y + (elo == 0u ? 1u : 0u);
    if (lane == 0) st2(rs, WS_CLR + b * 128, v2u{elo, ehi});
    STAMP(2);
  }

  if (roleB) {
    if (wv == 4) {
      v4u p[8];
      POLL_LOOP {
        cfence();
#pragma unroll
        for (int k = 0; k < 8; ++k) p[k] = ld4(rs, WS_QP + ((h * 8 + k) * 128 + 2 * lane) * 8);
        bool bad = false;
#pragma unroll
        for (int k = 0; k < 8; ++k) bad |= (p[k].y != elo) | (p[k].w != elo);
        if (!anyl(bad)) break;
      }
      cfence();
      const float q0 = ((u2f(p[0].x) + u2f(p[1].x)) + (u2f(p[2].x) + u2f(p[3].x))) + ((u2f(p[4].x) + u2f(p[5].x)) + (u2f(p[6].x) + u2f(p[7].x)));
      const float q1 = ((u2f(p[0].z) + u2f(p[1].z)) + (u2f(p[2].z) + u2f(p[3].z))) + ((u2f(p[4].z) + u2f(p[5].z)) + (u2f(p[6].z) + u2f(p[7].z)));
      *(LAS unsigned*)(L + L_QB + lane * 4) = pack2(q0, q1);
      STAMP(3);
    } else {
      drain();
    }
    const int rl = lane >> 3;
    const unsigned c0 = (unsigned)(((lane & 7) ^ (rl & 2)) * 16);
    const unsigned vo0 = (unsigned)((512 * sp_ + wv * 64 + rl) * 1024 + cj * 128);
    bar();
    {
      v4u uk[4], qb[4];
#pragma unroll
      for (int t = 0; t < 4; ++t) {
        uk[t] = *(LAS v4u*)(L + L_UK + wv * 4096 + l15 * 256 + (((4 * t + g) ^ l15) * 16));
        qb[t] = *(LAS v4u*)(L + L_QB + t * 64 + g * 16);
      }
      SB();
      f32x4 d = mfma32(uk[0], qb[0], Z4), e = mfma32(uk[1], qb[1], Z4);
      d = mfma32(uk[2], qb[2], d); e = mfma32(uk[3], qb[3], e);
      d = (d + e) * (0.08838834764831845f * 1.4426950408889634f);
      if (l15 == 0) *(LAS v2u*)(L + L_QO + wv * 32 + g * 8) = v2u{nosent(pack2(d[0], d[1])), nosent(pack2(d[2], d[3]))};
    }
    bar();
    if (wv == 0) {
      if (lane < 16) st4(rs, WS_QA + h * 1024 + j * 256 + lane * 16, *(LAS v4u*)(L + L_QO + lane * 16));
    } else {
      __builtin_amdgcn_s_sleep(1);
    }
    STAMP(4);
    if (wv == 7) wait_clr(rs, lane, elo, ehi);
#pragma unroll
    for (int i = 0; i < 8; ++i) dma16(ckv, vo0 + i * 8192 + (c0 ^ ((i & 1) << 6)), lbase + L_VT + (wv * 8 + i) * 1024);
    drain();
    bar();
    STAMP(5);
    const int kq = wv & 3, ht = wv >> 2;
    const int fsw = ((tq >> 1) & 1) | ((g & 1) << 1);
    v4u vf[4][4];
#pragma unroll
    for (int c = 0; c < 4; ++c) {
      LAS char* a = L + L_VT + (128 * kq + 8 * g + tq) * 128 + ((c ^ fsw) * 32) + tp * 8;
#pragma unroll
      for (int k = 0; k < 4; ++k) vf[k][c] = trfrag(a + k * 4096, a + k * 4096 + 512);
    }
    bar();
    const void* s2base;
    unsigned s2off, s2step, s2x;
    if (idx < 10) {
      s2base = wo; s2off = (unsigned)((512 * xcd + wv * 64 + rl) * 1280 + idx * 128); s2step = 8 * 1280; s2x = 64;
    } else {
      const int wi = (idx - 10) * 8 + wv, gi = wi / 12, part = wi - gi * 12;
      int col = xcd + 8 * gi; if (col >= 27) col = xcd;
      int row = part * 56; if (row > 640 - 64) row = 640 - 64;
      s2base = w1; s2off = (unsigned)((row + rl) * (DFF * 2) + col * 128); s2step = 8 * DFF * 2; s2x = 64;
    }
#pragma unroll
    for (int i = 0; i < WO_EARLY; ++i) dma16(s2base, s2off + i * s2step + (c0 ^ ((i & 1) * s2x)), lbase + L_VT + (wv * 8 + i) * 1024);
    {
      v4u pf[4];
      v2u mm[4];
      const int pb = WS_PB + ((sp_ * 16 + kq * 4) * 2 + ht) * 1024 + lane * 16;
      const int mo = WS_ST + (16 * sp_ + 4 * kq) * 256 + (16 * ht + l15) * 8;
      LAS char* rbw = L + L_RB + wv * 4096 + lane * 16;
      POLL_LOOP {
        cfence();
#pragma unroll
        for (int k = 0; k < 4; ++k) mm[k] = ld2(rs, mo + k * 256);
#pragma unroll
        for (int k = 0; k < 4; ++k) pf[k] = ld4(rs, pb + k * 2048);
        unsigned mx = 0u;
#pragma unroll
        for (int k = 0; k < 4; ++k) mx = max(max(mx, umax4(pf[k])), mm[k].x);
        if (!anyl(mx == SENT)) break;
      }
      cfence();
      STAMP(6);
      const float M = fmaxf(fmaxf(u2f(mm[0].x), u2f(mm[1].x)), fmaxf(u2f(mm[2].x), u2f(mm[3].x)));
      float sc[4];
#pragma unroll
      for (int k = 0; k < 4; ++k) sc[k] = __builtin_amdgcn_exp2f(u2f(mm[k].x) - M);
      if (lane < 16) *(LAS float*)(L + L_MW + (ht * 16 + lane) * 16 + kq * 4) = M;
#pragma unroll
      for (int c = 0; c < 4; ++c) {
        f32x4 d[4];
#pragma unroll
        for (int k = 0; k < 4; ++k) d[k] = mfma32(vf[k][c], pf[k], Z4);
        const f32x4 acc = (d[0] * sc[0] + d[1] * sc[1]) + (d[2] * sc[2] + d[3] * sc[3]);
        *(LAS f32x4*)(rbw + c * 1024) = acc;
      }
    }
    bar();
    {
      const f32x4 mw = *(LAS f32x4*)(L + L_MW + (ht * 16 + l15) * 16);
      f32x4 r[4];
#pragma unroll
      for (int q = 0; q < 4; ++q) r[q] = *(LAS f32x4*)(L + L_RB + (ht * 4 + q) * 4096 + kq * 1024 + lane * 16);
      const float M = fmaxf(fmaxf(mw[0], mw[1]), fmaxf(mw[2], mw[3]));
      const f32x4 acc = (r[0] * __builtin_amdgcn_exp2f(mw[0] - M) + r[1] * __builtin_amdgcn_exp2f(mw[1] - M)) +
                        (r[2] * __builtin_amdgcn_exp2f(mw[2] - M) + r[3] * __builtin_amdgcn_exp2f(mw[3] - M));
      st2(rs, WS_OP + (sp_ * 32 + cj * 4 + kq) * 1024 + (ht * 16 + l15) * 32 + g * 8,
          v2u{nosent(pack2(acc[0], acc[1])), nosent(pack2(acc[2], acc[3]))});
      STAMP(7);
    }
#pragma unroll
    for (int i = WO_EARLY; i < 6; ++i) dma16(s2base, s2off + i * s2step + (c0 ^ ((i & 1) * s2x)), lbase + L_VT + (wv * 8 + i) * 1024);
    waitvm<3>();
#pragma unroll
    for (int i = 6; i < 8; ++i) dma16(s2base, s2off + i * s2step + (c0 ^ ((i & 1) * s2x)), lbase + L_VT + (wv * 8 + i) * 1024);
    if (idx < 10) {
      float xr0 = 0.f, xr1 = 0.f;
      if (wv == 0 && xcd == 0) { const unsigned xv = ((const unsigned*)x)[idx * 32 + (lane & 31)]; xr0 = bflo(xv); xr1 = bfhi(xv); }
      drain();
      STAMP(8);
      v4u of[2][4];
#pragma unroll
      for (int n = 0; n < 4; ++n) {
        LAS char* a = L + L_VT + (64 * wv + 8 * g + tq) * 128 + ((n ^ fsw) * 32) + tp * 8;
        of[0][n] = trfrag(a, a + 512);
        of[1][n] = trfrag(a + 4096, a + 4096 + 512);
      }
      {
        v4u a0, a1;
        const int co = WS_CP + l15 * 8192 + (512 * xcd + 64 * wv + 8 * g) * 2;
        POLL_LOOP {
          cfence();
          a0 = ld4(rs, co); a1 = ld4(rs, co + 64);
          if (!anyl(max(umax4(a0), umax4(a1)) == SENT)) break;
        }
        cfence();
        STAMP(9);
        f32x4 d[4];
#pragma unroll
        for (int n = 0; n < 4; ++n) d[n] = mfma32(a0, of[0][n], Z4);
#pragma unroll
        for (int n = 0; n < 4; ++n) d[n] = mfma32(a1, of[1][n], d[n]);
        *(LAS float*)(L + L_RED + wv * 256 + lane * 4) = rs16(rs32(sum4(d[0]), sum4(d[2])), rs32(sum4(d[1]), sum4(d[3])));
      }
      bar();
      if (wv == 0) {
        f32x2 t[4];
#pragma unroll
        for (int w = 0; w < 4; ++w) t[w] = *(LAS f32x2*)(L + L_RED + ((lane >> 5) * 4 + w) * 256 + (lane & 31) * 8);
        const f32x2 u = (t[0] + t[1]) + (t[2] + t[3]);
        const float v0 = sum_x32(u.x) + xr0, v1 = sum_x32(u.y) + xr1;
        if (lane < 32) st1(rs, WS_HP + xcd * 1280 + idx * 128 + lane * 4, nosent(pack2(v0, v1)));
        STAMP(10);
      }
    } else if (wv == 7 && idx < 15) {
      const int tk = idx - 10, task = xcd * 5 + tk, rg = lane >> 2, cq = lane & 3;
      v2u hp;
      POLL_LOOP {
        cfence();
        hp = ld2(rs, WS_HP + (rg & 7) * 1280 + task * 32 + cq * 8);
        if (!anyl(max(hp.x, hp.y) == SENT)) break;
      }
      if (rg >= 8) hp = v2u{0u, 0u};
      v4u fr[7];
      const int fo = WS_FP + rg * 3072 + xcd * 384 + tk * 64 + cq * 16;
      const int fo6 = rg < 12 ? fo + 6 * 16 * 3072 : fo;
      const float m6 = rg < 12 ? 1.f : 0.f;
      POLL_LOOP {
        cfence();
#pragma unroll
        for (int k = 0; k < 6; ++k) fr[k] = ld4(rs, fo + k * 16 * 3072);
        fr[6] = ld4(rs, fo6);
        unsigned mx = 0u;
#pragma unroll
        for (int k = 0; k < 7; ++k) mx = max(mx, umax4(fr[k]));
        if (!anyl(mx == SENT)) break;
      }
      cfence();
      STAMP(14);
      f32x4 a = {bflo(hp.x), bfhi(hp.x), bflo(hp.y), bfhi(hp.y)};
#pragma unroll
      for (int k = 0; k < 6; ++k) a += __builtin_bit_cast(f32x4, fr[k]);
      if (rg < 12) a += __builtin_bit_cast(f32x4, fr[6]);
      (void)m6;
      float v = rs16(rs32(a[0], a[2]), rs32(a[1], a[3]));
      v += dppf<0x124>(v); v += dppf<0x128>(v);
      if (l15 < 4) out[task * 16 + l15 * 4 + g] = (u16)(pack2(v, v) & 0xFFFFu);
      if (task == 0 && lane == 0) st2(rs, WS_GEN, v2u{elo, ehi});
      STAMP(15);
    }
  } else {
    if (wv == 5 || wv == 6) {
      const int lw = wv - 5;
      unsigned vo[4];
#pragma unroll
      for (int i = 0; i < 4; ++i) {
        const int lam = lw * 32 + i * 8 + (lane >> 3);
        const int key = (lam & 32) + 8 * ((lam & 15) >> 2) + 4 * ((lam >> 4) & 1) + (lam & 3);
        vo[i] = (unsigned)((64 * id + key) * 1024 + (((lane & 7) ^ ((lam >> 1) & 7)) * 16));
      }
#pragma unroll
      for (int cg = 0; cg < KB1; ++cg)
#pragma unroll
        for (int i = 0; i < 4; ++i) dma16(ckv, vo[i] + cg * 128, lbase + L_KT + cg * 8192 + (lw * 4 + i) * 1024);
      waitvm<4>();
      bar();
      __builtin_amdgcn_s_sleep(2);
#pragma unroll
      for (int cg = KB1; cg < 8; ++cg)
#pragma unroll
        for (int i = 0; i < 4; ++i) dma16(ckv, vo[i] + cg * 128, lbase + L_KT + cg * 8192 + (lw * 4 + i) * 1024);
      waitvm<4 * (8 - KB1)>();
      bar();
      const unsigned uvo = (unsigned)((32 * idx + lw * 16) * 8192 + xcd * 1024 + lane * 16);
#pragma unroll
      for (int i = 0; i < 8; ++i) dma16(wuv, uvo + i * 8192, lbase + L_UV + (lw * 16 + i) * UV_STRIDE);
      waitvm<8>();
      bar();
      STAMP(5);
#pragma unroll
      for (int i = 8; i < 16; ++i) dma16(wuv, uvo + i * 8192, lbase + L_UV + (lw * 16 + i) * UV_STRIDE);
      if (W2_EARLY && has_ffn) {
#pragma unroll
        for (int i = 0; i < 10; ++i) dma16(w2, (unsigned)(ft * 20480 + (lw * 10 + i) * 1024 + lane * 16), lbase + L_W2 + (lw * 10 + i) * 1024);
        waitvm<10>();
      } else {
        drain();
      }
    } else if (wv < 4) {
      bar();
      STAMP(3);
      {
        v4u qf[8];
        const int qo = WS_QA + wv * 8192 + lane * 16;
        POLL_LOOP {
          cfence();
#pragma unroll
          for (int k = 0; k < 8; ++k) qf[k] = ld4(rs, qo + k * 1024);
          unsigned mx = 0u;
#pragma unroll
          for (int k = 0; k < 8; ++k) mx = max(mx, umax4(qf[k]));
          if (!anyl(mx == SENT)) break;
        }
        cfence();
#pragma unroll
        for (int k = 0; k < 8; ++k) *(LAS v4u*)(L + L_QA + (wv * 8 + k) * QA_STRIDE + lane * 16) = qf[k];
      }
      STAMP(4);
      bar();
      const int kt = wv >> 1, ht = wv & 1;
      v4u qf[16];
#pragma unroll
      for (int k = 0; k < 16; ++k) qf[k] = *(LAS v4u*)(L + L_QA + (ht * 16 + l15) * QA_STRIDE + k * 64 + g * 16);
      LAS char* ka = L + L_KT + (32 * kt + l15) * 128 + ((g ^ (l15 >> 1)) * 16);
      LAS char* kb = L + L_KT + (32 * kt + l15) * 128 + (((4 + g) ^ (l15 >> 1)) * 16);
      const int tile = id * 2 + kt;
      const int pbo = WS_PB + (tile * 2 + ht) * 1024 + lane * 16, sto = WS_ST + tile * 256 + (ht * 16 + l15) * 8;
      f32x4 s0 = Z4, s1 = Z4;
      {
        v4u kf[KB1][4];
#pragma unroll
        for (int cg = 0; cg < KB1; ++cg) {
          kf[cg][0] = *(LAS v4u*)(ka + cg * 8192); kf[cg][1] = *(LAS v4u*)(ka + cg * 8192 + 2048);
          kf[cg][2] = *(LAS v4u*)(kb + cg * 8192); kf[cg][3] = *(LAS v4u*)(kb + cg * 8192 + 2048);
        }
        SB();
#pragma unroll
        for (int cg = 0; cg < KB1; ++cg) {
          s0 = mfma32(kf[cg][0], qf[2 * cg], s0); s1 = mfma32(kf[cg][1], qf[2 * cg], s1);
          s0 = mfma32(kf[cg][2], qf[2 * cg + 1], s0); s1 = mfma32(kf[cg][3], qf[2 * cg + 1], s1);
        }
        SB();
      }
      bar();
      STAMP(5);
      {
        v4u kf[8 - KB1][4];
#pragma unroll
        for (int cg = KB1; cg < 8; ++cg) {
          kf[cg - KB1][0] = *(LAS v4u*)(ka + cg * 8192); kf[cg - KB1][1] = *(LAS v4u*)(ka + cg * 8192 + 2048);
          kf[cg - KB1][2] = *(LAS v4u*)(kb + cg * 8192); kf[cg - KB1][3] = *(LAS v4u*)(kb + cg * 8192 + 2048);
        }
        SB();
#pragma unroll
        for (int cg = KB1; cg < 8; ++cg) {
          s0 = mfma32(kf[cg - KB1][0], qf[2 * cg], s0); s1 = mfma32(kf[cg - KB1][1], qf[2 * cg], s1);
          s0 = mfma32(kf[cg - KB1][2], qf[2 * cg + 1], s0); s1 = mfma32(kf[cg - KB1][3], qf[2 * cg + 1], s1);
        }
      }
      float m = fmaxf(fmaxf(fmaxf(s0[0], s0[1]), fmaxf(s0[2], s0[3])), fmaxf(fmaxf(s1[0], s1[1]), fmaxf(s1[2], s1[3])));
      m = max_x32(max_x16(m));
      v4u pk;
      pk.x = nosent(pack2(__builtin_amdgcn_exp2f(s0[0] - m), __builtin_amdgcn_exp2f(s0[1] - m)));
      pk.y = nosent(pack2(__builtin_amdgcn_exp2f(s0[2] - m), __builtin_amdgcn_exp2f(s0[3] - m)));
      pk.z = nosent(pack2(__builtin_amdgcn_exp2f(s1[0] - m), __builtin_amdgcn_exp2f(s1[1] - m)));
      pk.w = nosent(pack2(__builtin_amdgcn_exp2f(s1[2] - m), __builtin_amdgcn_exp2f(s1[3] - m)));
      st4(rs, pbo, pk);
      float ls = ((bflo(pk.x) + bfhi(pk.x)) + (bflo(pk.y) + bfhi(pk.y))) + ((bflo(pk.z) + bfhi(pk.z)) + (bflo(pk.w) + bfhi(pk.w)));
      ls = sum_x32(sum_x16(ls));
      if (lane < 16) st2(rs, sto, v2u{nosent(f2u(m)), nosent(f2u(ls))});
      STAMP(6);
    } else {
      if (wv == 7) wait_clr(rs, lane, elo, ehi);
      bar(); bar(); bar();
      if (cj == 0) {
        const int hh = wv == 7 ? 16 : 0;
        v2u mm[4];
        const int mo = WS_ST + (16 * sp_ + 4 * g) * 256 + (hh + l15) * 8;
        POLL_LOOP {
          cfence();
#pragma unroll
          for (int k = 0; k < 4; ++k) mm[k] = ld2(rs, mo + k * 256);
          unsigned mx = 0u;
#pragma unroll
          for (int k = 0; k < 4; ++k) mx = max(mx, max(mm[k].x, mm[k].y));
          if (!anyl(mx == SENT)) break;
        }
        cfence();
        const float M = max_x32(max_x16(fmaxf(fmaxf(u2f(mm[0].x), u2f(mm[1].x)), fmaxf(u2f(mm[2].x), u2f(mm[3].x)))));
        float Ls = 0.f;
#pragma unroll
        for (int k = 0; k < 4; ++k) Ls += __builtin_amdgcn_exp2f(u2f(mm[k].x) - M) * u2f(mm[k].y);
        Ls = sum_x32(sum_x16(Ls));
        if (lane < 16) st2(rs, WS_S2 + sp_ * 256 + (hh + lane) * 8, v2u{nosent(f2u(M)), nosent(f2u(Ls))});
        STAMP(6);
      }
    }
    bar();
    STAMP(7);
    v4u uf[4];
#pragma unroll
    for (int n = 0; n < 4; ++n) {
      LAS char* a = L + L_UV + (8 * g + tq) * UV_STRIDE + wv * 128 + n * 32 + tp * 8;
      uf[n] = trfrag(a, a + 4 * UV_STRIDE);
    }
    const int uh = xcd * 4 + (wv >> 1);
    float wsc[4];
    {
      v2u ml;
      POLL_LOOP {
        cfence();
        ml = ld2(rs, WS_S2 + l15 * 256 + uh * 8);
        if (!anyl(max(ml.x, ml.y) == SENT)) break;
      }
      cfence();
      const float ms = u2f(ml.x);
      float M = fmaxf(ms, dppf<0xB1>(ms)); M = fmaxf(M, dppf<0x4E>(M));
      M = fmaxf(M, dppf<0x124>(M)); M = fmaxf(M, dppf<0x128>(M));
      const float e = __builtin_amdgcn_exp2f(ms - M);
      const float wl = e * __builtin_amdgcn_rcpf(row_sum(e * u2f(ml.y)));
#pragma unroll
      for (int k = 0; k < 4; ++k) wsc[k] = u2f((unsigned)__builtin_amdgcn_ds_bpermute((4 * g + k) * 4, (int)f2u(wl)));
    }
    STAMP(8);
    {
      v4u a0;
      const int oo = WS_OP + (l15 * 32 + idx * 2 + (g >> 1)) * 1024 + uh * 32 + (g & 1) * 16;
      const int cpo = WS_CP + idx * 8192 + xcd * 1024 + wv * 128 + lane * 2;
      POLL_LOOP {
        cfence();
        a0 = ld4(rs, oo);
        if (!anyl(umax4(a0) == SENT)) break;
      }
      cfence();
      STAMP(9);
      f32x4 d[4];
#pragma unroll
      for (int n = 0; n < 4; ++n) d[n] = mfma32(a0, uf[n], Z4);
      float c[4];
#pragma unroll
      for (int n = 0; n < 4; ++n) c[n] = (d[n][0] * wsc[0] + d[n][1] * wsc[1]) + (d[n][2] * wsc[2] + d[n][3] * wsc[3]);
      const float cv = rs16(rs32(c[0], c[2]), rs32(c[1], c[3]));
      const float cn = dppf<0xB1>(cv);
      if ((lane & 1) == 0) st1(rs, cpo, nosent(pack2(cv, cn)));
      STAMP(10);
    }
    if (!has_ffn) return;
    bar();
    if (wv < 5) {
      const unsigned fo1 = (unsigned)(((lane >> 1) + wv * 128) * (DFF * 2) + ft * 32 + (lane & 1) * 16);
#pragma unroll
      for (int i = 0; i < 4; ++i) dma16(w1, fo1 + i * 32 * DFF * 2, lbase + L_W1 + (wv * 4 + i) * 1024);
    } else if (!W2_EARLY && wv < 7) {
#pragma unroll
      for (int i = 0; i < 10; ++i) dma16(w2, (unsigned)(ft * 20480 + ((wv - 5) * 10 + i) * 1024 + lane * 16), lbase + L_W2 + ((wv - 5) * 10 + i) * 1024);
    }
    drain();
    v4u w1f[4];
    if (wv < 5) {
      LAS char* a = L + L_W1 + wv * 4096 + (8 * g + tq) * 32 + tp * 8;
#pragma unroll
      for (int k = 0; k < 4; ++k) w1f[k] = trfrag(a + k * 1024, a + k * 1024 + 128);
      f32x4 cp = mfma32(ones, w1f[0], Z4);
#pragma unroll
      for (int k = 1; k < 4; ++k) cp = mfma32(ones, w1f[k], cp);
      if (lane < 16) *(LAS float*)(L + L_CS + wv * 64 + lane * 4) = cp[0];
    }
    bar();
    STAMP(11);
    v2u w2f[5];
#pragma unroll
    for (int k = 0; k < 5; ++k) w2f[k] = trread(L + L_W2 + (4 * g + tq) * 1280 + ((wv * 5 + k) * 16 + 4 * tp) * 2);
    f32x4 cs = Z4;
#pragma unroll
    for (int w = 0; w < 5; ++w) cs += *(LAS f32x4*)(L + L_CS + w * 64 + g * 16);
    const int fpo = WS_FP + ft * 3072 + wv * 384 + l15 * 64 + g * 16;
    if (wv < 5) {
      v4u h0, h1;
      const int ho = WS_HP + (l15 & 7) * 1280 + wv * 256 + (l15 >> 3) * 64 + g * 16;
      POLL_LOOP {
        cfence();
        h0 = ld4(rs, ho); h1 = ld4(rs, ho + 128);
        if (!anyl(max(umax4(h0), umax4(h1)) == SENT)) break;
      }
      cfence();
      STAMP(12);
      f32x4 gm = mfma32(h0, h0, Z4);
      f32x4 da = mfma32(h0, w1f[0], Z4), db = mfma32(h0, w1f[1], Z4);
      gm = mfma32(h1, h1, gm);
      f32x4 sm = mfma32(h0, ones, Z4);
      da = mfma32(h1, w1f[2], da); db = mfma32(h1, w1f[3], db);
      sm = mfma32(h1, ones, sm);
      const float gs = row_sum(((l15 < 8) == (g < 2)) ? sum4(gm) : 0.f);
      const float hv = g < 2 ? sum4(da) : sum4(db);
      const float r = rs16(rs32(hv, gs), rs32(hv, sum4(sm)));
      if (lane < 16) *(LAS float*)(L + L_FR + wv * 64 + lane * 4) = r;
      if (lane == 32 || lane == 48) *(LAS float*)(L + L_FS + (g - 2) * 32 + wv * 4) = r;
    }
    bar();
    {
      f32x4 hw = Z4;
#pragma unroll
      for (int w = 0; w < 5; ++w) hw += *(LAS f32x4*)(L + L_FR + w * 64 + g * 16);
      const f32x4 qa = *(LAS f32x4*)(L + L_FS), sa = *(LAS f32x4*)(L + L_FS + 32);
      const float q4 = *(LAS float*)(L + L_FS + 16), s4 = *(LAS float*)(L + L_FS + 48);
      const float mean = (sum4(sa) + s4) * (1.0f / DM);
      const float var = fmaxf((sum4(qa) + q4) * (1.0f / DM) - mean * mean, 0.f);
      const float rstd = __builtin_amdgcn_rsqf(var + 1e-5f);
      float f[4];
#pragma unroll
      for (int k = 0; k < 4; ++k) f[k] = fmaxf((hw[k] - mean * cs[k]) * rstd, 0.f);
      const v2u fb = {pack2(f[0], f[1]), pack2(f[2], f[3])};
      f32x4 o = Z4;
#pragma unroll
      for (int k = 0; k < 5; ++k) {
        const f32x4 d = mfma16(w2f[k], fb, Z4);
        if (l15 == k) o = d;
      }
      if (l15 < 5) st4(rs, fpo, v4u{nosent(f2u(o[0])), nosent(f2u(o[1])), nosent(f2u(o[2])), nosent(f2u(o[3]))});
      STAMP(13);
    }
  }
}

extern "C" void kernel_launch(void* const* d_in, const int* in_sizes, int n_in, void* d_out, int out_size, void* d_ws, size_t ws_size,
                              hipStream_t stream) {
  mla_block_kernel<<<256, 512, 0, stream>>>((const u16*)d_in[0], (const u16*)d_in[1], (const u16*)d_in[2], (const u16*)d_in[3],
                                            (const u16*)d_in[4], (const u16*)d_in[5], (const u16*)d_in[7], (const u16*)d_in[8],
                                            (u16*)d_out, (char*)d_ws);
}
```

```cpp
#include <hip/hip_runtime.h>
#include <hip/hip_bf16.h>

typedef unsigned short u16;
typedef unsigned long long u64;
typedef unsigned v4u __attribute__((ext_vector_type(4)));
typedef unsigned v2u __attribute__((ext_vector_type(2)));
typedef float f32x4 __attribute__((ext_vector_type(4)));
typedef float f32x2 __attribute__((ext_vector_type(2)));
typedef short s16x8 __attribute__((ext_vector_type(8)));
typedef short s16x4 __attribute__((ext_vector_type(4)));
typedef __bf16 bf16x2_t __attribute__((ext_vector_type(2)));
typedef unsigned short u16x2 __attribute__((ext_vector_type(2)));
#define LAS __attribute__((address_space(3)))
#define DI __device__ __forceinline__

constexpr int K0 = 2, K5 = 4, KP = 4, VP = 8, AP = 4, WO_EARLY = 3, WOG = 4;
constexpr int UK_SLEEP = 1, K_SLEEP = 8, W2_SLEEP = 1, W1_SLEEP = 3, W2L_SLEEP = 6, AN_SLEEP = 16;
constexpr int D0 = 90, C1 = 250, C1S = 40, C2 = C1 + 100, C3 = C2 + 115, C4 = C3 + 108, WSTOP = 70;
constexpr int CODE_KB = 14;

constexpr int DM = 640, DFF = 1728;
constexpr unsigned SENT = 0xFFFFFFFFu;
constexpr unsigned SPIN_CAP = 1u << 20;
constexpr int AUX_SC1 = 16;

constexpr int WS_GEN = 0;
constexpr int WS_OUT = 4096, OSTR = 9600;
constexpr int O_CP = 4096;
constexpr int O_HP = 4096;
constexpr int O_FP = 5120;
constexpr int O_QA = 8192;
constexpr int O_FLAG = 8448;
constexpr int O_ST = 8576;
constexpr int O_S2 = 8576, O_B1 = 8832;
constexpr int O_B2 = 9088, O_B0 = 9216;
constexpr int O_QP = 9344;
constexpr int WS_END = WS_OUT + 256 * OSTR;
#define OBOX(role_a, xc, ix) (WS_OUT + (((role_a) * 16 + (ix)) * 8 + (xc)) * OSTR)

constexpr int L_RED = 0, L_XL = 2560, L_MW = 4608, L_FR = 5120, L_FS = 5440, L_CS = 5504, L_OG = 0, L_WS = 4608;
constexpr int L_T0 = 8192;
constexpr int L_WQ = 73728;
constexpr int L_UK = 107008, L_QG = 140800, L_EP = 5888;
constexpr int L_RB = 73728, L_X = 106496, L_TR = 140800;
constexpr int UV_STRIDE = 1056, QA_STRIDE = 1040;
constexpr int L_QA = 73728, L_UV = 107008, L_W2 = 140800, L_W1 = L_T0, L_TRA = L_T0 + 32768;
constexpr int LDS_BYTES = 161280;

DI float u2f(unsigned u) { return __builtin_bit_cast(float, u); }
DI unsigned f2u(float f) { return __builtin_bit_cast(unsigned, f); }
DI float bflo(unsigned u) { return u2f(u << 16); }
DI float bfhi(unsigned u) { return u2f(u & 0xFFFF0000u); }
DI unsigned pack2(float a, float b) {
  f32x2 v = {a, b};
  bf16x2_t p = __builtin_convertvector(v, bf16x2_t);
  return __builtin_bit_cast(unsigned, p);
}
DI unsigned pfx(v4u v, int i) { return v[i]; }
DI unsigned nosent(unsigned u) { return u < 0xFFFFFFFEu ? u : 0xFFFFFFFEu; }
template <int CTRL> DI float dppf(float v) {
  return u2f((unsigned)__builtin_amdgcn_update_dpp(0, (int)f2u(v), CTRL, 0xf, 0xf, true));
}
DI float rs16(float a, float b) { auto r = __builtin_amdgcn_permlane16_swap(f2u(a), f2u(b), false, false); unsigned x = r[0], y = r[1]; return u2f(x) + u2f(y); }
DI float rs32(float a, float b) { auto r = __builtin_amdgcn_permlane32_swap(f2u(a), f2u(b), false, false); unsigned x = r[0], y = r[1]; return u2f(x) + u2f(y); }
DI float sum_x16(float v) { return rs16(v, v); }
DI float sum_x32(float v) { return rs32(v, v); }
DI float fmx(float a, float b) { return __builtin_elementwise_maximum(a, b); }
DI float max_x16(float v) { auto r = __builtin_amdgcn_permlane16_swap(f2u(v), f2u(v), false, false); unsigned a = r[0], b = r[1]; return fmx(u2f(a), u2f(b)); }
DI float max_x32(float v) { auto r = __builtin_amdgcn_permlane32_swap(f2u(v), f2u(v), false, false); unsigned a = r[0], b = r[1]; return fmx(u2f(a), u2f(b)); }
DI float row_sum(float v) { v += dppf<0xB1>(v); v += dppf<0x4E>(v); v += dppf<0x124>(v); v += dppf<0x128>(v); return v; }
DI float wave_sum(float v) { return sum_x32(sum_x16(row_sum(v))); }
DI float sum4(f32x4 d) { return (d[0] + d[1]) + (d[2] + d[3]); }
DI f32x4 mfma32(v4u a, v4u b, f32x4 c) {
  return __builtin_amdgcn_mfma_f32_16x16x32_bf16(__builtin_bit_cast(s16x8, a), __builtin_bit_cast(s16x8, b), c, 0, 0, 0);
}
DI f32x4 mfma16(v2u a, v2u b, f32x4 c) {
  return __builtin_amdgcn_mfma_f32_16x16x16bf16_1k(__builtin_bit_cast(s16x4, a), __builtin_bit_cast(s16x4, b), c, 0, 0, 0);
}
DI v2u trread(LAS char* p) { return __builtin_bit_cast(v2u, __builtin_amdgcn_ds_read_tr16_b64_v4i16((LAS s16x4*)p)); }
DI v4u trfrag(LAS char* p0, LAS char* p1) { v2u a = trread(p0), b = trread(p1); return v4u{a.x, a.y, b.x, b.y}; }
DI void bar() { asm volatile("s_waitcnt lgkmcnt(0)\n\ts_barrier" ::: "memory"); }
DI void drain() { asm volatile("s_waitcnt vmcnt(0)" ::: "memory"); }
template <int N> DI void waitvm() { asm volatile("s_waitcnt vmcnt(%0)" ::"n"(N) : "memory"); }
DI void cfence() { asm volatile("" ::: "memory"); }
DI u64 rtime() { return __builtin_amdgcn_s_memrealtime(); }
#define PIN(x) asm volatile("" : "+v"(x))
#define SB() __builtin_amdgcn_sched_barrier(0)
DI void dma16(const void* base, unsigned voff, unsigned lds) {
  lds = __builtin_amdgcn_readfirstlane(lds);
  asm volatile("s_mov_b32 m0, %2\n\ts_nop 0\n\tglobal_load_lds_dwordx4 %0, %1" ::"v"(voff), "s"(base), "s"(lds) : "memory");
}
DI void dma16x4(const void* base, unsigned v0, unsigned v1, unsigned v2, unsigned v3, unsigned lds) {
  lds = __builtin_amdgcn_readfirstlane(lds);
  asm volatile("s_mov_b32 m0, %5\n\ts_nop 0\n\tglobal_load_lds_dwordx4 %0, %4\n\tglobal_load_lds_dwordx4 %1, %4 offset:1024\n\t"
               "global_load_lds_dwordx4 %2, %4 offset:2048\n\tglobal_load_lds_dwordx4 %3, %4 offset:3072"
               ::"v"(v0), "v"(v1), "v"(v2), "v"(v3), "s"(base), "s"(lds) : "memory");
}
DI void dma16n(const void* base, unsigned voff, unsigned lds) {
  lds = __builtin_amdgcn_readfirstlane(lds);
  asm volatile("s_mov_b32 m0, %2\n\ts_nop 0\n\tglobal_load_lds_dwordx4 %0, %1 nt" ::"v"(voff), "s"(base), "s"(lds) : "memory");
}
DI void dma16x4n(const void* base, unsigned v0, unsigned v1, unsigned v2, unsigned v3, unsigned lds) {
  lds = __builtin_amdgcn_readfirstlane(lds);
  asm volatile("s_mov_b32 m0, %5\n\ts_nop 0\n\tglobal_load_lds_dwordx4 %0, %4 nt\n\tglobal_load_lds_dwordx4 %1, %4 offset:1024 nt\n\t"
               "global_load_lds_dwordx4 %2, %4 offset:2048 nt\n\tglobal_load_lds_dwordx4 %3, %4 offset:3072 nt"
               ::"v"(v0), "v"(v1), "v"(v2), "v"(v3), "s"(base), "s"(lds) : "memory");
}
typedef __amdgpu_buffer_rsrc_t rsrc_t;
DI v4u ld4(rsrc_t r, int off) { return __builtin_amdgcn_raw_buffer_load_b128(r, off, 0, AUX_SC1); }
DI v2u ld2(rsrc_t r, int off) { return __builtin_amdgcn_raw_buffer_load_b64(r, off, 0, AUX_SC1); }
DI void st4(rsrc_t r, int off, v4u v) { __builtin_amdgcn_raw_buffer_store_b128(v, r, off, 0, AUX_SC1); }
DI void st2(rsrc_t r, int off, v2u v) { __builtin_amdgcn_raw_buffer_store_b64(v, r, off, 0, AUX_SC1); }
DI void st1(rsrc_t r, int off, unsigned v) { __builtin_amdgcn_raw_buffer_store_b32(v, r, off, 0, AUX_SC1); }
DI unsigned umax4(v4u v) { return max(max(v.x, v.y), max(v.z, v.w)); }
DI bool anyl(bool p) { return __builtin_amdgcn_ballot_w64(p) != 0ull; }
#define POLL_LOOP _Pragma("unroll 1") for (unsigned sp = 0; sp < SPIN_CAP; ++sp)


DI void wait_bell(rsrc_t rs, int off) {
  POLL_LOOP {
    cfence();
    unsigned v = __builtin_amdgcn_raw_buffer_load_b32(rs, 0, off, AUX_SC1);
    if (__builtin_amdgcn_readfirstlane(v) != SENT) break;
  }
  cfence();
}
DI void wait_tick(unsigned t) { while ((int)((unsigned)rtime() - t) < 0) {} }
DI unsigned anchor_time(rsrc_t rs, int lane, unsigned elo, unsigned ehi) {
  v4u a, c;
  POLL_LOOP {
    cfence();
    a = ld4(rs, WS_OUT + (128 + lane) * OSTR + O_B0); c = ld4(rs, WS_OUT + (192 + lane) * OSTR + O_B0);
    if (!anyl((a.x != elo) | (a.y != ehi) | (c.x != elo) | (c.y != ehi))) break;
    __builtin_amdgcn_s_sleep(8);
  }
  cfence();
  const unsigned now = (unsigned)rtime();
  float age = -(float)min(min(now - a.z, now - c.z), 100000u);
  age = fmx(age, dppf<0xB1>(age)); age = fmx(age, dppf<0x4E>(age)); age = fmx(age, dppf<0x124>(age)); age = fmx(age, dppf<0x128>(age));
  age = max_x32(max_x16(age));
  return now - (unsigned)(-age);
}
DI LAS char* lp(unsigned a) { return (LAS char*)(size_t)a; }
template <class T> DI T lds(unsigned a, int off) { return *(LAS T*)(lp(a) + off); }
template <class T> DI void sts(unsigned a, int off, T v) { *(LAS T*)(lp(a) + off) = v; }
DI v4u trf(unsigned a, int off, int step) { return trfrag(lp(a) + off, lp(a) + off + step); }

__global__ void __launch_bounds__(512, 2)
mla_block_kernel(const u16* __restrict__ x, const u16* __restrict__ ckv, const u16* __restrict__ wq, const u16* __restrict__ wuk,
                 const u16* __restrict__ wuv, const u16* __restrict__ wo, const u16* __restrict__ w1, const u16* __restrict__ w2,
                 u16* __restrict__ out, char* ws) {
  __shared__ __attribute__((aligned(16))) char smem[LDS_BYTES];
  const unsigned lb = (unsigned)(size_t)(LAS char*)smem;
  const int tid = threadIdx.x, lane = tid & 63, wv = __builtin_amdgcn_readfirstlane(tid >> 6);
  const int rl = lane >> 3, l7 = lane & 7;
  const int b = blockIdx.x, xcd = b & 7, slot = b >> 3, idx = slot & 15;
  const int h = (xcd >> 2) * 16 + idx, dj = slot >> 4, cq = xcd & 3;
  const unsigned c0 = (unsigned)((l7 ^ (rl & 2)) * 16);
  if (wv < 5) {
    const unsigned q0 = (unsigned)((160 * cq + 32 * wv + rl) * 8192 + h * 256 + dj * 128) + c0, q1 = (q0 ^ 64u) + (65536 - 1024);
    dma16x4n(wq, q0, q1, q0 + 2 * (65536 - 1024), q1 + 2 * (65536 - 1024), lb + L_WQ + wv * 4096);
  }
  SB();
  u64 pc0;
  asm volatile("s_getpc_b64 %0" : "=s"(pc0));
  asm volatile("" ::"s"(x), "s"(ckv), "s"(wuk), "s"(wuv), "s"(wo), "s"(w1), "s"(w2), "s"(out), "s"(ws));
  const int l15 = lane & 15, g = lane >> 4, tq = l15 >> 2, tp = lane & 3;
  const bool roleB = slot < 16;
  const rsrc_t rs = __builtin_amdgcn_make_buffer_rsrc((void*)ws, 0, WS_END, 0x00020000);
  const v4u S4 = {SENT, SENT, SENT, SENT};
  const v4u ones = {0x3F803F80u, 0x3F803F80u, 0x3F803F80u, 0x3F803F80u};
  const f32x4 Z4 = {0.f, 0.f, 0.f, 0.f};
  const int ob = WS_OUT + b * OSTR;
  if (wv < 5) {
    dma16(x, (unsigned)(cq * 320 + wv * 64 + g * 16), lb + L_WQ + 20480 + wv * 1024);
  } else if (wv == 7) {
    dma16(ws, (unsigned)(WS_GEN + tp * 16), lb + 7168);
    dma16(x, (unsigned)(lane * 16), lb + L_XL);
    dma16(x, (unsigned)(1024 + l15 * 16), lb + L_XL + 1024);
  }
  {
    int co_ = ob + (wv == 7 ? 8192 : wv * 1024);
    if (roleB && wv >= 5 && wv < 7) co_ = WS_END;
    __builtin_amdgcn_raw_buffer_store_b128(S4, rs, lane * 16, co_, AUX_SC1);
  }
  SB();
  asm volatile("s_barrier" ::: "memory");
  __builtin_amdgcn_s_setprio(3);
  const int id = xcd * 16 + idx;
  const int sp_ = xcd * 2 + (idx >> 3), cj = idx & 7;
  const int fgrp = xcd + 8 * (idx >> 2), ft = fgrp * 4 + (idx & 3);
  const bool has_ffn = fgrp < 27;
  const int dbo = lane == 0 ? ob : WS_END;
  unsigned elo = 0, ehi = 0;
  const int fsw = ((tq >> 1) & 1) | ((g & 1) << 1);
  const unsigned trb = (unsigned)((8 * g + tq) * 128 + tp * 8);
  const unsigned kk0 = (unsigned)(id * 65536 + wv * 128 + (8 * (rl >> 2) + (rl & 3)) * 1024 + ((l7 ^ (rl >> 1)) * 16));
#define K_PIECE(i) dma16(ckv, (kk0 ^ (((i) & 1) << 6)) + (32 * ((i) >> 2) + 16 * ((i) & 1) + 4 * (((i) >> 1) & 1)) * 1024, lb + L_T0 + (wv * 8 + (i)) * 1024)
  const unsigned v0 = (unsigned)(rl * 8192 + ((l7 ^ (rl >> 1)) * 16));
  const unsigned v1 = (v0 ^ 64u) + (65536 - 1024), v2 = v0 + 2 * (65536 - 1024), v3 = v1 + 2 * (65536 - 1024);
  const char* ukb = (const char*)wuk + (unsigned)((128 * cq + 64 * (wv - 5)) * 8192 + h * 256);
  float mean = 0.f;
  if (wv < 5) {
    unsigned ta[4];
#pragma unroll
    for (int n = 0; n < 4; ++n) { ta[n] = lb + L_WQ + wv * 4096 + trb + ((n ^ fsw) * 32); PIN(ta[n]); }
    unsigned qw = lb + wv * 512 + lane * 4, xw = lb + L_WQ + 20480 + wv * 1024 + lane * 16;
    PIN(qw); PIN(xw);
    waitvm<1>();
    v4u xa = lds<v4u>(xw, 0);
    v4u bf[4];
#pragma unroll
    for (int n = 0; n < 4; ++n) bf[n] = trf(ta[n], 0, 512);
    SB();
    if (lane & 1) xa = ones;
    f32x4 d[4];
#pragma unroll
    for (int n = 0; n < 4; ++n) d[n] = mfma32(xa, bf[n], Z4);
    sts<float>(qw, L_RED, g < 2 ? (g == 0 ? d[0][0] : d[1][0]) : (g == 2 ? d[2][0] : d[3][0]));
    sts<float>(qw, L_RED + 256, g < 2 ? (g == 0 ? d[0][1] : d[1][1]) : (g == 2 ? d[2][1] : d[3][1]));
    drain();
    if (!roleB) {
#pragma unroll
      for (int i = 0; i < K0; ++i) K_PIECE(i);
    }
  } else if (wv < 7) {
    if (wv == 5 && slot < CODE_KB) dma16((const void*)(pc0 & ~255ull), (unsigned)(slot * 1024 + lane * 16), lb + 6144);
    __builtin_amdgcn_s_setprio(0);
    __builtin_amdgcn_s_sleep(UK_SLEEP);
    const unsigned ud = lb + (roleB ? L_UK + (wv - 5) * 16384 + 8192 : L_UV);
    if (roleB) {
      dma16x4n(ukb + 128, v0, v1, v2, v3, ud);
      dma16x4n(ukb + 128 + 262144, v0, v1, v2, v3, ud + 4096);
    } else {
      dma16x4(ukb, v0, v1, v2, v3, ud);
      dma16x4(ukb + 262144, v0, v1, v2, v3, ud);
      __builtin_amdgcn_s_sleep(K_SLEEP);
#pragma unroll
      for (int i = 0; i < K5; ++i) K_PIECE(i);
    }
  } else {
    waitvm<1>();
    unsigned xs[5];
#pragma unroll
    for (int i = 0; i < 5; ++i) xs[i] = lds<unsigned>(lb + lane * 4, L_XL + i * 256);
    const v2u gen = lds<v2u>(lb, 7168);
    SB();
    const float sm = ((bflo(xs[0]) + bfhi(xs[0])) + (bflo(xs[1]) + bfhi(xs[1]))) + ((bflo(xs[2]) + bfhi(xs[2])) + (bflo(xs[3]) + bfhi(xs[3]))) + (bflo(xs[4]) + bfhi(xs[4]));
    mean = wave_sum(sm) * (1.0f / DM);
    elo = gen.x + 1u; ehi = gen.y + (elo == 0u ? 1u : 0u);
    sts<v4u>(lb, L_EP, v4u{elo, f2u(mean), ehi, 0u});
    drain();
    st2(rs, dbo + O_FLAG, v2u{elo, ehi});
    mean = (lane & 16) ? -mean : 1.0f;
    PIN(mean);
  }
  unsigned qr = lb + (lane & 31) * 16;
  int qpo = lane < 16 ? ob + O_QP + lane * 16 : WS_END;
  PIN(qr); PIN(qpo);
  bar();
  if (wv == 7) {
    f32x4 p[5];
#pragma unroll
    for (int w = 0; w < 5; ++w) p[w] = lds<f32x4>(qr, L_RED + w * 512);
    SB();
    const f32x4 q = (((p[0] + p[1]) + (p[2] + p[3])) + p[4]) * mean;
    st4(rs, qpo, v4u{pack2(sum_x16(q[0]), sum_x16(q[1])), elo, pack2(sum_x16(q[2]), sum_x16(q[3])), elo});
    SB();
    if (!roleB) st4(rs, ob + 7168 + lane * 16, S4);
  }
  __builtin_amdgcn_s_setprio(0);

  if (roleB) {
    if (wv == 5 || wv == 6) {
      const int lw = wv - 5;
      __builtin_amdgcn_s_sleep(2);
      dma16x4(ukb, v0, v1, v2, v3, lb + L_UK + lw * 16384);
      dma16x4(ukb + 262144, v0, v1, v2, v3, lb + L_UK + lw * 16384 + 4096);
      const v2u ep = lds<v2u>(lb, L_EP);
      const unsigned tag = ep.x;
      float var = 0.f;
#pragma unroll
      for (int i = 0; i < 5; ++i) { const unsigned xv = lds<unsigned>(lb + lane * 4, L_XL + i * 256); float a = bflo(xv) - u2f(ep.y), c = bfhi(xv) - u2f(ep.y); var += a * a + c * c; }
      float qscale = __builtin_amdgcn_rsqf(wave_sum(var) * (1.0f / DM) + 1e-5f) * (0.08838834764831845f * 1.4426950408889634f);
      const int qg = OBOX(0, (xcd & 4) + g, idx) + O_QP + l15 * 16;
      unsigned gw = lb + L_QG + lw * 1088 + g * 272 + l15 * 8, gr = lb + L_QG + lw * 1088 + (l15 & 3) * 272 + g * 16;
      const unsigned ua = lb + L_UK + lw * 16384 + l15 * 128 + ((g ^ (l15 >> 1)) * 16), ub = ua ^ 64;
      int qao = (lane & 1) ? WS_END : ob + O_QA + lw * 128 + lane * 2;
      PIN(qscale); PIN(gw); PIN(gr); PIN(qao);
      drain();
      v4u uk[4][4];
#pragma unroll
      for (int t = 0; t < 4; ++t)
#pragma unroll
        for (int n = 0; n < 4; ++n) uk[n][t] = lds<v4u>((t & 1) ? ub : ua, (t >> 1) * 8192 + n * 2048);
      v4u p0, p1;
      POLL_LOOP {
        cfence();
        p0 = ld4(rs, qg); p1 = ld4(rs, qg + 128 * OSTR);
        if (!anyl((p0.y != tag) | (p0.w != tag) | (p1.y != tag) | (p1.w != tag))) break;
      }
      cfence();
      sts<v2u>(gw, 0, v2u{p0.x, p0.z});
      sts<v2u>(gw, 128, v2u{p1.x, p1.z});
      cfence();
      v4u qa[4];
#pragma unroll
      for (int t = 0; t < 4; ++t) qa[t] = lds<v4u>(gr, t * 64);
      SB();
      if (wv == 5) st1(rs, dbo + O_B1, 1u);
      f32x4 d[4] = {Z4, Z4, Z4, Z4};
#pragma unroll
      for (int t = 0; t < 4; ++t)
#pragma unroll
        for (int n = 0; n < 4; ++n) d[n] = mfma32(qa[t], uk[n][t], d[n]);
      const float e0 = sum4(d[0]), e1 = sum4(d[1]), e2 = sum4(d[2]), e3 = sum4(d[3]);
      const float qv = (g < 2 ? (g == 0 ? e0 : e1) : (g == 2 ? e2 : e3)) * qscale;
      st1(rs, qao, nosent(pack2(qv, dppf<0xB1>(qv))));
    }
    bar();
    {
      const unsigned k0 = (unsigned)((512 * sp_ + wv * 64 + rl) * 1024 + cj * 128) + c0;
#pragma unroll
      for (int i = 0; i < 8; ++i) {
        dma16(ckv, (k0 ^ ((i & 1) << 6)) + i * 8192, lb + L_T0 + (wv * 8 + i) * 1024);
        waitvm<VP - 1>();
      }
    }
    const void* s2base;
    unsigned s2off, s2step;
    if (idx < 10) {
      s2base = wo; s2off = (unsigned)((512 * xcd + wv * 64 + rl) * 1280 + idx * 128) + c0; s2step = 8 * 1280;
    } else {
      const int wi = (idx - 10) * 8 + wv, gi = wi / 12, part = wi - gi * 12;
      int col = xcd + 8 * gi; if (col >= 27) col = xcd;
      int row = part * 56; if (row > 640 - 64) row = 640 - 64;
      s2base = w1; s2off = (unsigned)((row + rl) * (DFF * 2) + col * 128) + c0; s2step = 8 * DFF * 2;
    }
#define WO_PIECE(i) dma16(s2base, (s2off ^ (((i) & 1) << 6)) + (i) * s2step, lb + ((i) < 4 ? L_X : L_T0 - 4096) + (wv * 4 + (i)) * 1024)
    waitvm<WOG>();
#pragma unroll
    for (int i = 0; i < WO_EARLY; ++i) WO_PIECE(i);
    waitvm<WO_EARLY>();
    bar();
    const int kq = wv & 3, ht = wv >> 2;
    v4u vf[4][4];
#pragma unroll
    for (int c = 0; c < 4; ++c) {
      const unsigned a = lb + L_T0 + kq * 16384 + trb + ((c ^ fsw) * 32);
#pragma unroll
      for (int k = 0; k < 4; ++k) vf[k][c] = trf(a, k * 4096, 512);
    }
    const v4u ep4 = lds<v4u>(lb, L_EP);
    unsigned rbr = lb + L_RB + ht * 16384 + kq * 1024 + lane * 16, mwr = lb + (ht * 16 + l15) * 16;
    int opo = ob + kq * 1024 + (ht * 16 + l15) * 32 + g * 8;
    {
      v4u pf[4];
      v2u mm;
      const int pb = OBOX(1, xcd, (idx & 8) + 2 * kq) + ht * 1024 + lane * 16;
      const int mo = OBOX(1, xcd, (idx & 8) + 2 * kq + (g >> 1)) + O_ST + (g & 1) * 256 + (16 * ht + l15) * 8;
      unsigned rbw = lb + L_RB + wv * 4096 + lane * 16, mww = mwr + kq * 4;
      int s2o = lane < 16 ? ob + O_S2 + lane * 8 : WS_END;
      PIN(s2o); PIN(rbw); PIN(mww); PIN(rbr); PIN(mwr); PIN(opo);
      {
        v4u bl;
        const int bo = OBOX(1, xcd, (idx & 8) + 2 * kq + (lane & 1)) + O_B0;
        POLL_LOOP {
          cfence();
          bl = ld4(rs, bo);
          if (!anyl((bl.x != ep4.x) | (bl.y != ep4.z))) break;
        }
        cfence();
        const unsigned t0 = __builtin_amdgcn_readlane(bl.z, 0), t1 = __builtin_amdgcn_readlane(bl.z, 1);
        for (;;) {
          const unsigned now = (unsigned)rtime();
          if (now - t0 >= (unsigned)D0 && now - t1 >= (unsigned)D0) break;
        }
      }
      POLL_LOOP {
        cfence();
#pragma unroll
        for (int k = 0; k < 4; ++k) pf[k] = ld4(rs, pb + (k >> 1) * (8 * OSTR) + (k & 1) * 2048);
        mm = ld2(rs, mo);
        unsigned mx = max(mm.x, mm.y);
#pragma unroll
        for (int k = 0; k < 4; ++k) mx = max(mx, umax4(pf[k]));
        if (!anyl(mx == SENT)) break;
      }
      cfence();
      float mt[4];
      {
        auto r = __builtin_amdgcn_permlane16_swap(mm.x, mm.x, false, false);
        const unsigned ev = r[0], od = r[1];
        auto r0 = __builtin_amdgcn_permlane32_swap(ev, ev, false, false);
        auto r1 = __builtin_amdgcn_permlane32_swap(od, od, false, false);
        const unsigned a0 = r0[0], a2 = r0[1], a1 = r1[0], a3 = r1[1];
        mt[0] = u2f(a0); mt[1] = u2f(a1); mt[2] = u2f(a2); mt[3] = u2f(a3);
      }
      const float M = fmx(fmx(mt[0], mt[1]), fmx(mt[2], mt[3]));
      f32x4 d[4] = {Z4, Z4, Z4, Z4};
#pragma unroll
      for (int k = 0; k < 4; ++k) {
        const unsigned sh = (unsigned)fminf(M - mt[k], 255.f) * 0x00800080u;
        const u16x2 s2 = __builtin_bit_cast(u16x2, sh);
        v4u ps;
        ps.x = __builtin_bit_cast(unsigned, __builtin_elementwise_sub_sat(__builtin_bit_cast(u16x2, pfx(pf[k], 0)), s2));
        ps.y = __builtin_bit_cast(unsigned, __builtin_elementwise_sub_sat(__builtin_bit_cast(u16x2, pfx(pf[k], 1)), s2));
        ps.z = __builtin_bit_cast(unsigned, __builtin_elementwise_sub_sat(__builtin_bit_cast(u16x2, pfx(pf[k], 2)), s2));
        ps.w = __builtin_bit_cast(unsigned, __builtin_elementwise_sub_sat(__builtin_bit_cast(u16x2, pfx(pf[k], 3)), s2));
#pragma unroll
        for (int c = 0; c < 4; ++c) d[c] = mfma32(vf[k][c], ps, d[c]);
        SB();
      }
      sts<float>(mww, L_MW, M);
      if (wv == cj) {
        const float Lw = sum_x32(sum_x16(__builtin_amdgcn_exp2f(u2f(mm.x) - M) * u2f(mm.y)));
        st2(rs, s2o, v2u{nosent(f2u(M)), nosent(f2u(Lw))});
      }
      SB();
#pragma unroll
      for (int c = 0; c < 4; ++c) sts<f32x4>(rbw, c * 1024, d[c]);
    }
    bar();
    {
      const f32x4 mw = lds<f32x4>(mwr, L_MW);
      f32x4 r[4];
#pragma unroll
      for (int q = 0; q < 4; ++q) r[q] = lds<f32x4>(rbr, q * 4096);
      SB();
      const float M = fmx(fmx(mw[0], mw[1]), fmx(mw[2], mw[3]));
      const f32x4 acc = (r[0] * __builtin_amdgcn_exp2f(mw[0] - M) + r[1] * __builtin_amdgcn_exp2f(mw[1] - M)) +
                        (r[2] * __builtin_amdgcn_exp2f(mw[2] - M) + r[3] * __builtin_amdgcn_exp2f(mw[3] - M));
      st2(rs, opo, v2u{nosent(pack2(acc[0], acc[1])), nosent(pack2(acc[2], acc[3]))});
    }
    unsigned tanc = 0;
#pragma unroll
    for (int i = WO_EARLY; i < 8; ++i) WO_PIECE(i);
    POLL_LOOP {
      cfence();
      tanc = __builtin_amdgcn_readfirstlane(__builtin_amdgcn_raw_buffer_load_b32(rs, 0, OBOX(1, xcd, idx) + O_B2, AUX_SC1));
      if (tanc != SENT) break;
    }
    cfence();
    if ((unsigned)rtime() - tanc > 100000u) tanc = (unsigned)rtime() - 100000u;
    if (idx < 10) {
      float xr0 = 0.f, xr1 = 0.f;
      if (xcd == 0) { const unsigned xv = lds<unsigned>(lb + idx * 128 + (lane & 31) * 4, L_XL); xr0 = bflo(xv); xr1 = bfhi(xv); }
      drain();
      v4u of[2][4];
#pragma unroll
      for (int n = 0; n < 4; ++n) {
        const unsigned a = lb + wv * 4096 + trb + ((n ^ fsw) * 32);
        of[0][n] = trf(a + L_X, 0, 512);
        of[1][n] = trf(a, L_T0, 512);
      }
      unsigned rdr = lb + (lane >> 5) * 1024 + (lane & 31) * 8;
      int hpo = ob + O_HP + wv * 128 + lane * 4;
      {
        v4u a0, a1;
        const int co = OBOX(1, xcd, rl) + O_CP + wv * 128 + l7 * 16;
        unsigned tw0 = lb + L_TR + wv * 2048 + rl * 128 + ((l7 ^ (rl >> 1)) * 16), tw1 = tw0 ^ 64;
        unsigned tr0 = lb + L_TR + wv * 2048 + l15 * 128 + ((g ^ (l15 >> 1)) * 16), tr1 = tr0 ^ 64;
        unsigned rdw = lb + wv * 256 + lane * 4;
        PIN(tw0); PIN(tw1); PIN(tr0); PIN(tr1); PIN(rdw); PIN(rdr); PIN(hpo); PIN(xr0); PIN(xr1);
        wait_tick(tanc + C2);
        POLL_LOOP {
          cfence();
          a0 = ld4(rs, co); a1 = ld4(rs, co + 64 * OSTR);
          if (!anyl(max(umax4(a0), umax4(a1)) == SENT)) break;
        }
        cfence();
        sts<v4u>(tw0, 0, a0);
        sts<v4u>(tw1, 1024, a1);
        cfence();
        a0 = lds<v4u>(tr0, 0);
        a1 = lds<v4u>(tr1, 0);
        f32x4 d[4];
#pragma unroll
        for (int n = 0; n < 4; ++n) d[n] = mfma32(a0, of[0][n], Z4);
#pragma unroll
        for (int n = 0; n < 4; ++n) d[n] = mfma32(a1, of[1][n], d[n]);
        sts<float>(rdw, L_RED, rs16(rs32(sum4(d[0]), sum4(d[2])), rs32(sum4(d[1]), sum4(d[3]))));
      }
      bar();
      {
        f32x2 t[4];
#pragma unroll
        for (int w = 0; w < 4; ++w) t[w] = lds<f32x2>(rdr, L_RED + w * 256);
        SB();
        const f32x2 u = (t[0] + t[1]) + (t[2] + t[3]);
        const float v0 = sum_x32(u.x) + xr0, v1 = sum_x32(u.y) + xr1;
        if (lane < 32) st1(rs, hpo, nosent(pack2(v0, v1)));
      }
    } else if (idx < 15) {
      const int tk = idx - 10, task = xcd * 5 + tk, rg = lane >> 2, c4 = lane & 3;
      wait_tick(tanc + C3 + 20);
      if (wv < 5) {
        v4u h0, h1;
        const int ho = OBOX(0, rl, 2 * wv) + O_HP + xcd * 128 + l7 * 16;
        const unsigned tw = lb + L_TR + wv * 2304 + rl * 144 + l7 * 16;
        const unsigned tr = lb + L_TR + wv * 2304 + (l15 & 7) * 144 + (l15 >> 3) * 64 + g * 16;
        POLL_LOOP {
          cfence();
          h0 = ld4(rs, ho); h1 = ld4(rs, ho + 8 * OSTR);
          if (!anyl(max(umax4(h0), umax4(h1)) == SENT)) break;
        }
        cfence();
        sts<v4u>(tw, 0, h0);
        sts<v4u>(tw, 1152, h1);
        cfence();
        h0 = lds<v4u>(tr, 0);
        h1 = lds<v4u>(tr, 1152);
        f32x4 gm = mfma32(h0, h0, Z4), sm = mfma32(h0, ones, Z4);
        gm = mfma32(h1, h1, gm); sm = mfma32(h1, ones, sm);
        const float s2 = wave_sum(((l15 < 8) == (g < 2)) ? sum4(gm) : 0.f), s1 = sum_x32(sum_x16(sum4(sm)));
        sts<f32x2>(lb + wv * 8, L_FS, f32x2{s1, s2});
      }
      bar();
      if (wv != 7) return;
      const int blk = task >> 2;
      v2u hp = lds<v2u>(lb + L_TR + (blk >> 1) * 2304 + (blk & 1) * 1152 + (rg & 7) * 144 + (task & 3) * 32 + c4 * 8, 0);
      if (rg >= 8) hp = v2u{0u, 0u};
      float s1 = 0.f, s2 = 0.f;
#pragma unroll
      for (int w = 0; w < 5; ++w) { const f32x2 t = lds<f32x2>(lb + w * 8, L_FS); s1 += t.x; s2 += t.y; }
      const float hmean = s1 * (1.0f / DM);
      float rstd = __builtin_amdgcn_rsqf(fmaxf(s2 * (1.0f / DM) - hmean * hmean, 0.f) + 1e-5f);
      float hres = rs16(rs32(bflo(hp.x), bflo(hp.y)), rs32(bfhi(hp.x), bfhi(hp.y)));
      hres += dppf<0x124>(hres); hres += dppf<0x128>(hres);
      v4u fr[7];
      const int fo = WS_OUT + (128 + rg) * OSTR + O_FP + xcd * 384 + tk * 64 + c4 * 16;
      const int fo6 = rg < 12 ? OBOX(1, rg % 3, 12 + rg / 3) + O_FP + xcd * 384 + tk * 64 + c4 * 16 : WS_END;
      unsigned oo = (unsigned)((task * 16 + l15 * 4 + g) * 2);
      PIN(oo); PIN(rstd); PIN(hres);
      wait_tick(tanc + C4);
      POLL_LOOP {
        cfence();
#pragma unroll
        for (int k = 0; k < 6; ++k) fr[k] = ld4(rs, fo + k * 16 * OSTR);
        fr[6] = ld4(rs, fo6);
        unsigned mx = 0u;
#pragma unroll
        for (int k = 0; k < 7; ++k) mx = max(mx, umax4(fr[k]));
        if (!anyl(mx == SENT)) break;
      }
      cfence();
      const f32x4 a = (__builtin_bit_cast(f32x4, fr[0]) + (__builtin_bit_cast(f32x4, fr[1]) + __builtin_bit_cast(f32x4, fr[2]))) +
          ((__builtin_bit_cast(f32x4, fr[3]) + __builtin_bit_cast(f32x4, fr[4])) + (__builtin_bit_cast(f32x4, fr[5]) + __builtin_bit_cast(f32x4, fr[6])));
      float v = rs16(rs32(a[0], a[2]), rs32(a[1], a[3]));
      v += dppf<0x124>(v); v += dppf<0x128>(v);
      v = v * rstd + hres;
      if (l15 < 4) *(u16*)((char*)out + oo) = (u16)pack2(v, v);
      if (task == 0 && lane == 0) st2(rs, WS_GEN, v2u{elo, ehi});
    }
  } else {
#pragma unroll
    for (int i = 0; i < 8; ++i) {
      if (i >= (wv < 5 ? K0 : (wv < 7 ? K5 : 0))) {
        K_PIECE(i);
        if (wv < 4) waitvm<KP - 1>();
      }
    }
    unsigned qfr = lb + L_QA + ((wv & 1) * 16 + l15) * QA_STRIDE + g * 16;
    int pbo = ob + wv * 1024 + lane * 16, sto = lane < 16 ? ob + O_ST + wv * 128 + lane * 8 : WS_END;
    unsigned ka = lb + L_T0 + (32 * (wv >> 1) + l15) * 128 + ((g ^ (l15 >> 1)) * 16), kb = ka ^ 64;
    PIN(qfr); PIN(pbo); PIN(sto); PIN(ka); PIN(kb);
    const v4u ep4 = lds<v4u>(lb, L_EP);
    if (wv >= 4) {
      POLL_LOOP {
        cfence();
        const v2u f0 = ld2(rs, OBOX(0, (wv & 2) * 2 + g, 8 * (wv & 1) + l7) + O_FLAG);
        if (!anyl((f0.x != ep4.x) | (f0.y != ep4.z))) break;
        __builtin_amdgcn_s_sleep(8);
      }
      wait_bell(rs, OBOX(0, (wv & 2) * 2 + cq, 8 * (wv & 1) + cj) + O_B1);
      const unsigned qsw = lb + L_QA + (wv & 3) * 8 * QA_STRIDE + lane * 16;
      const int qo = OBOX(0, (wv & 2) * 2 + g, 8 * (wv & 1)) + O_QA + l15 * 16;
      v4u qa[8];
      POLL_LOOP {
        cfence();
#pragma unroll
        for (int k = 0; k < 8; ++k) qa[k] = __builtin_amdgcn_raw_buffer_load_b128(rs, qo, k * 8 * OSTR, AUX_SC1);
        unsigned mx = 0u;
#pragma unroll
        for (int k = 0; k < 8; ++k) { mx = max(mx, umax4(qa[k])); sts<v4u>(qsw, k * QA_STRIDE, qa[k]); }
        if (!anyl(mx == SENT)) break;
      }
      cfence();
    }
    drain();
    bar();
    if (wv < 4) {
      v4u qf[16], kf[8][4];
      f32x4 s0 = Z4, s1 = Z4;
#pragma unroll
      for (int c = 0; c < 10; ++c) {
        if (c < 8) {
          qf[2 * c] = lds<v4u>(qfr, c * 128);
          kf[c][0] = lds<v4u>(ka, c * 8192); kf[c][1] = lds<v4u>(ka, c * 8192 + 2048);
          qf[2 * c + 1] = lds<v4u>(qfr, c * 128 + 64);
          kf[c][2] = lds<v4u>(kb, c * 8192); kf[c][3] = lds<v4u>(kb, c * 8192 + 2048);
          SB();
        }
        if (c >= 2) {
          const int e = c - 2;
          s0 = mfma32(kf[e][0], qf[2 * e], s0); s1 = mfma32(kf[e][1], qf[2 * e], s1);
          s0 = mfma32(kf[e][2], qf[2 * e + 1], s0); s1 = mfma32(kf[e][3], qf[2 * e + 1], s1);
          SB();
        }
      }
      float m = fmx(fmx(fmx(s0[0], s0[1]), fmx(s0[2], s0[3])), fmx(fmx(s1[0], s1[1]), fmx(s1[2], s1[3])));
      m = __builtin_ceilf(max_x32(max_x16(m)));
      v4u pk;
      pk.x = nosent(pack2(__builtin_amdgcn_exp2f(s0[0] - m), __builtin_amdgcn_exp2f(s0[1] - m)));
      pk.y = nosent(pack2(__builtin_amdgcn_exp2f(s0[2] - m), __builtin_amdgcn_exp2f(s0[3] - m)));
      pk.z = nosent(pack2(__builtin_amdgcn_exp2f(s1[0] - m), __builtin_amdgcn_exp2f(s1[1] - m)));
      pk.w = nosent(pack2(__builtin_amdgcn_exp2f(s1[2] - m), __builtin_amdgcn_exp2f(s1[3] - m)));
      st4(rs, pbo, pk);
      SB();
      const f32x4 lsv = mfma32(ones, pk, Z4);
      st2(rs, sto, v2u{nosent(f2u(m)), nosent(f2u(lsv[0]))});
      if (wv == 3) { __builtin_amdgcn_s_sleep(AN_SLEEP); const unsigned ta_ = nosent(anchor_time(rs, lane, ep4.x, ep4.z)); sts<unsigned>(lb, L_EP + 16, ta_); st1(rs, dbo + O_B2, ta_); }
    } else {
      const unsigned ta3 = (unsigned)rtime();
      if (wv == 7) st4(rs, dbo + O_B0, v4u{elo, ehi, ta3, 0u});
      __builtin_amdgcn_s_sleep(W2_SLEEP);
      const unsigned uvo = (unsigned)((32 * idx + (wv - 4) * 8) * 8192 + xcd * 1024 + lane * 16);
#pragma unroll
      for (int i = 0; i < 8; ++i) { dma16n(wuv, uvo + i * 8192, lb + L_UV + ((wv - 4) * 8 + i) * UV_STRIDE); waitvm<AP - 1>(); }
      const unsigned woo = (unsigned)((512 * xcd + 64 * (idx >> 1) + 8 * WO_EARLY) * 1280 + (idx & 1) * (8 - WO_EARLY) * 5120 + lane * 16);
#pragma unroll 1
      for (int t = wv - 4; t < 5 * (8 - WO_EARLY); t += 4) {
        if ((unsigned)rtime() - ta3 > (unsigned)(C1 - WSTOP)) break;
        dma16(wo, woo + t * 1024, lb + L_W2 + wv * 1024); waitvm<AP - 1>();
      }
    }
    drain();
    bar();
    const unsigned tanc = __builtin_amdgcn_readfirstlane(lds<unsigned>(lb, L_EP + 16));
    v4u uf[4];
#pragma unroll
    for (int n = 0; n < 4; ++n) uf[n] = trf(lb + L_UV + (8 * g + tq) * UV_STRIDE + wv * 128 + tp * 8, n * 32, 4 * UV_STRIDE);
    {
      int cpo = ob + O_CP + wv * 128 + lane * 2;
      unsigned ogr = lb + l15 * 272 + (g >> 1) * 128 + (wv >> 1) * 32 + (g & 1) * 16;
      unsigned wsr = lb + (wv >> 1) * 64 + g * 16;
      PIN(cpo); PIN(ogr); PIN(wsr);
      if (wv < 4) {
        v4u a0;
        const int oo = OBOX(0, 2 * wv + (g >> 1), 8 * (g & 1) + (idx >> 1)) + (2 * (idx & 1) + (rl & 1)) * 1024 + xcd * 128 + l7 * 16;
        unsigned ogw = lb + (4 * wv + g) * 272 + l15 * 16;
        PIN(ogw);
        wait_tick(tanc + C1);
        POLL_LOOP {
          cfence();
          a0 = ld4(rs, oo);
          if (!anyl(umax4(a0) == SENT)) break;
        }
        cfence();
        sts<v4u>(ogw, L_OG, a0);
      } else {
        v2u ml;
        const int so = OBOX(0, rl, 8 * ((lane >> 2) & 1) + (xcd >> 2) * 4 + tp) + O_S2 + ((xcd & 3) * 4 + wv - 4) * 8;
        unsigned wsw = lb + (wv - 4) * 64 + (lane >> 2) * 4;
        PIN(wsw);
        wait_tick(tanc + C1 - C1S);
        POLL_LOOP {
          cfence();
          ml = ld2(rs, so);
          if (!anyl(max(ml.x, ml.y) == SENT)) break;
          __builtin_amdgcn_s_sleep(1);
        }
        cfence();
        const float mq = u2f(ml.x);
        float ms = fmx(mq, dppf<0xB1>(mq)); ms = fmx(ms, dppf<0x4E>(ms));
        float M = fmx(ms, dppf<0x124>(ms)); M = fmx(M, dppf<0x128>(M));
        M = max_x32(max_x16(M));
        const float wl = __builtin_amdgcn_exp2f(ms - M) * __builtin_amdgcn_rcpf(wave_sum(__builtin_amdgcn_exp2f(mq - M) * u2f(ml.y)));
        sts<float>(wsw, L_WS, wl);
      }
      bar();
      const v4u a0 = lds<v4u>(ogr, L_OG);
      const f32x4 wsc = lds<f32x4>(wsr, L_WS);
      f32x4 d[4];
#pragma unroll
      for (int n = 0; n < 4; ++n) d[n] = mfma32(a0, uf[n], Z4);
      float c[4];
#pragma unroll
      for (int n = 0; n < 4; ++n) c[n] = (d[n][0] * wsc[0] + d[n][1] * wsc[1]) + (d[n][2] * wsc[2] + d[n][3] * wsc[3]);
      const float cv = rs16(rs32(c[0], c[2]), rs32(c[1], c[3]));
      const float cn = dppf<0xB1>(cv);
      if ((lane & 1) == 0) st1(rs, cpo, nosent(pack2(cv, cn)));
    }
    if (!has_ffn) return;
    if (wv < 5) {
      __builtin_amdgcn_s_sleep(W1_SLEEP);
      const unsigned fo1 = (unsigned)(((lane >> 1) + wv * 128) * (DFF * 2) + ft * 32 + (lane & 1) * 16);
#pragma unroll
      for (int i = 0; i < 4; ++i) dma16(w1, fo1 + i * 32 * DFF * 2, lb + L_W1 + (wv * 4 + i) * 1024);
    } else if (wv < 7) {
      __builtin_amdgcn_s_sleep(W2L_SLEEP);
#pragma unroll
      for (int i = 0; i < 10; ++i) dma16n(w2, (unsigned)(ft * 20480 + ((wv - 5) * 10 + i) * 1024 + lane * 16), lb + L_W2 + ((wv - 5) * 10 + i) * 1024);
    }
    drain();
    v4u w1f[4];
    if (wv < 5) {
#pragma unroll
      for (int k = 0; k < 4; ++k) w1f[k] = trf(lb + wv * 4096 + (8 * g + tq) * 32 + tp * 8, L_W1 + k * 1024, 128);
      f32x4 cp = mfma32(ones, w1f[0], Z4);
#pragma unroll
      for (int k = 1; k < 4; ++k) cp = mfma32(ones, w1f[k], cp);
      if (lane < 16) sts<float>(lb + wv * 64 + lane * 4, L_CS, cp[0]);
    }
    bar();
    v2u w2f[5];
#pragma unroll
    for (int k = 0; k < 5; ++k) w2f[k] = trread(lp(lb + L_W2 + (4 * g + tq) * 1280 + wv * 160 + tp * 8) + k * 32);
    unsigned g16 = lb + g * 16;
    float csn = 0.f;
#pragma unroll
    for (int w = 0; w < 5; ++w) csn += lds<float>(lb + l15 * 4, L_CS + w * 64);
    csn *= -1.0f / DM;
    int fpo = l15 < 5 ? ob + O_FP + wv * 384 + l15 * 64 + g * 16 : WS_END;
    unsigned mk[5];
#pragma unroll
    for (int k = 0; k < 5; ++k) { mk[k] = l15 == k ? 0xFFFFFFFFu : 0u; PIN(mk[k]); }
    PIN(csn); PIN(fpo); PIN(g16);
    if (wv < 5) {
      v4u h0, h1;
      const int ho = OBOX(0, rl, 2 * wv) + O_HP + xcd * 128 + l7 * 16;
      unsigned tw = lb + L_TRA + wv * 2304 + rl * 144 + l7 * 16;
      unsigned tr = lb + L_TRA + wv * 2304 + (l15 & 7) * 144 + (l15 >> 3) * 64 + g * 16;
      unsigned fw = lb + wv * 64 + l15 * 4;
      v4u on = ones;
      PIN(tw); PIN(tr); PIN(fw); PIN(on);
      wait_tick(tanc + C3);
      POLL_LOOP {
        cfence();
        h0 = ld4(rs, ho); h1 = ld4(rs, ho + 8 * OSTR);
        sts<v4u>(tw, 0, h0);
        sts<v4u>(tw, 1152, h1);
        if (!anyl(max(umax4(h0), umax4(h1)) == SENT)) break;
      }
      cfence();
      h0 = lds<v4u>(tr, 0);
      h1 = lds<v4u>(tr, 1152);
      SB();
      f32x4 sm = mfma32(h0, on, Z4);
      f32x4 da = mfma32(h0, w1f[0], Z4), db = mfma32(h0, w1f[1], Z4);
      sm = mfma32(h1, on, sm);
      da = mfma32(h1, w1f[2], da); db = mfma32(h1, w1f[3], db);
      SB();
      const float z = (g < 2 ? sum4(da) : sum4(db)) + sum4(sm) * csn;
      sts<float>(fw, L_FR, sum_x32(sum_x16(z)));
    }
    bar();
    {
      f32x4 hp[5];
#pragma unroll
      for (int w = 0; w < 5; ++w) hp[w] = lds<f32x4>(g16, L_FR + w * 64);
      SB();
      const f32x4 hw = ((hp[0] + hp[1]) + (hp[2] + hp[3])) + hp[4];
      const v2u fb = {pack2(fmx(hw[0], 0.f), fmx(hw[1], 0.f)), pack2(fmx(hw[2], 0.f), fmx(hw[3], 0.f))};
      f32x4 o = Z4, o2 = Z4;
#pragma unroll
      for (int k = 0; k < 5; ++k) {
        const v2u bm = {fb.x & mk[k], fb.y & mk[k]};
        if (k & 1) o2 = mfma16(w2f[k], bm, o2); else o = mfma16(w2f[k], bm, o);
      }
      o += o2;
      st4(rs, fpo, v4u{nosent(f2u(o[0])), nosent(f2u(o[1])), nosent(f2u(o[2])), nosent(f2u(o[3]))});
    }
  }
}

extern "C" void kernel_launch(void* const* d_in, const int* in_sizes, int n_in, void* d_out, int out_size, void* d_ws, size_t ws_size,
                              hipStream_t stream) {
  mla_block_kernel<<<256, 512, 0, stream>>>((const u16*)d_in[0], (const u16*)d_in[1], (const u16*)d_in[2], (const u16*)d_in[3],
                                            (const u16*)d_in[4], (const u16*)d_in[5], (const u16*)d_in[7], (const u16*)d_in[8],
                                            (u16*)d_out, (char*)d_ws);
}
```
